```python
import torch

def _softmax(x, axis=-1):
    return torch.softmax(x, dim=axis)
def _gelu(x):
    return torch.nn.functional.gelu(x, approximate="tanh")
def _silu(x):
    return torch.nn.functional.silu(x)
def _sigmoid(x):
    return torch.sigmoid(x)
def _layernorm(x, axis=-1, eps=1e-5):
    m = x.mean(dim=axis, keepdim=True)
    v = x.var(dim=axis, keepdim=True, unbiased=False)
    return (x - m) / torch.sqrt(v + eps)
def _scatter_add(tgt, idx, src, axis=0):
    return tgt.clone().index_add_(axis, idx, src)
def _segment_sum(src, seg, n):
    out = torch.zeros((n,) + src.shape[1:], dtype=src.dtype)
    return out.index_add_(0, seg, src)
def _pad(x, pw):
    flat = tuple(v for pair in reversed(pw) for v in pair)
    return torch.nn.functional.pad(x, flat)

kernel_name = 'interleave_layers__grouped_kv_mqa__dense_activation_gelu__prompt'

def setup_inputs():

    _w = (lambda a: (lambda t: t.to(torch.bfloat16) if t.is_floating_point() else t)(torch.as_tensor(a)))
    def _A_setup():
        B, S, D, H = 1, 2048, 512, 32
        Dh = D // H
        return {
            "x": _w((lambda *s: torch.randn(*s, dtype=torch.bfloat16) * 0.05)(B, S, D)),
            "wq": _w((lambda *s: torch.randn(*s, dtype=torch.bfloat16) * 0.05)(D, H * Dh)),
            "wk": _w((lambda *s: torch.randn(*s, dtype=torch.bfloat16) * 0.05)(D, Dh)),
            "wv": _w((lambda *s: torch.randn(*s, dtype=torch.bfloat16) * 0.05)(D, Dh)),
            "wo": _w((lambda *s: torch.randn(*s, dtype=torch.bfloat16) * 0.05)(H * Dh, D)),
        }
    _A_in = _A_setup()

    def _B_setup():
        B, S, D, F = 1, 2048, 512, 1376
        return {
            "x": _w((lambda *s: torch.randn(*s, dtype=torch.bfloat16) * 0.05)(B, S, D)),
            "w1": _w((lambda *s: torch.randn(*s, dtype=torch.bfloat16) * 0.8)(D, F) * (D ** -0.5)),
            "b1": _w((lambda *s: torch.randn(*s, dtype=torch.bfloat16) * 12.8)(F) * 0.01),
            "w2": _w((lambda *s: torch.randn(*s, dtype=torch.bfloat16) * 0.2)(F, D) * (F ** -0.5)),
            "b2": _w((lambda *s: torch.randn(*s, dtype=torch.bfloat16) * 1.6)(D) * 0.01),
        }
    _B_in = _B_setup()

    return {
        "A_x": _A_in["x"],
        "A_wq": _A_in["wq"],
        "A_wk": _A_in["wk"],
        "A_wv": _A_in["wv"],
        "A_wo": _A_in["wo"],
        "B_x": _B_in["x"],
        "B_w1": _B_in["w1"],
        "B_b1": _B_in["b1"],
        "B_w2": _B_in["w2"],
        "B_b2": _B_in["b2"],
    }

def reference(A_x, A_wq, A_wk, A_wv, A_wo, B_x, B_w1, B_b1, B_w2, B_b2):
    def _A_ref(x, wq, wk, wv, wo):
        B, S, D = x.shape
        H = 32
        Dh = D // H
        q = (lambda x, *d: x.permute(*d))((x @ wq).reshape(B, S, H, Dh), 0, 2, 1, 3)
        k = x @ wk
        v = x @ wv
        scores = torch.einsum("bhqd,bkd->bhqk", q, k) * (Dh ** -0.5)
        p = _softmax(scores, axis=-1)
        o = torch.einsum("bhqk,bkd->bhqd", p, v)
        return (lambda x, *d: x.permute(*d))(o, 0, 2, 1, 3).reshape(B, S, H * Dh) @ wo

    def _B_ref(x, w1, b1, w2, b2):
        h = torch.einsum("bsd,df->bsf", x, w1) + b1
        h = _gelu(h)
        return torch.einsum("bsf,fd->bsd", h, w2) + b2

    x = A_x
    x = x + _A_ref(_layernorm(x, axis=-1), A_wq, A_wk, A_wv, A_wo)
    x = x + _B_ref(_layernorm(x, axis=-1), B_w1, B_b1, B_w2, B_b2)
    x = x + _A_ref(_layernorm(x, axis=-1), A_wq, A_wk, A_wv, A_wo)
    x = x + _B_ref(_layernorm(x, axis=-1), B_w1, B_b1, B_w2, B_b2)
    return x
```

```cpp
#include <hip/hip_runtime.h>
#include <hip/hip_bf16.h>
#include <stdint.h>

typedef __attribute__((ext_vector_type(8))) short bf16x8;
typedef __attribute__((ext_vector_type(16))) float f32x16;
typedef __attribute__((ext_vector_type(4))) unsigned u32x4;
typedef __attribute__((ext_vector_type(2))) unsigned u32x2;
typedef __attribute__((ext_vector_type(2))) float f32x2;
typedef __attribute__((ext_vector_type(4))) float f32x4;
typedef __bf16 bf16x2_t __attribute__((ext_vector_type(2)));
#define LAS __attribute__((address_space(3)))
#define GAS __attribute__((address_space(1)))
#define DI __device__ __forceinline__
#define MFMA(a, b, c) __builtin_amdgcn_mfma_f32_32x32x16_bf16((a), (b), (c), 0, 0, 0)

constexpr int S_ = 2048, D_ = 512, F_ = 1376;
constexpr int NTB = 64, KSD = 32, KSF = 86, NTF = 43;

constexpr size_t MB = 1024 * 1024;
constexpr size_t OFF_X0F = 0;
constexpr size_t OFF_X1F = 2 * MB;
constexpr size_t OFF_X2F = 4 * MB;
constexpr size_t OFF_QF = 6 * MB;
constexpr size_t OFF_OF = 8 * MB;
constexpr size_t OFF_HF = 10 * MB;
constexpr size_t OFF_KF = 16 * MB;
constexpr size_t OFF_VF = OFF_KF + 65536;
constexpr size_t OFF_WQKV = 17 * MB;
constexpr size_t OFF_WO = 18 * MB;
constexpr size_t OFF_W1 = 19 * MB;
constexpr size_t OFF_W2 = 21 * MB;
constexpr size_t OFF_CSQ = 23 * MB;
constexpr size_t OFF_CS1 = OFF_CSQ + 4096;
constexpr size_t OFF_SP0 = 24 * MB;
constexpr size_t OFF_SP1 = OFF_SP0 + 262144;
constexpr size_t OFF_SP2 = OFF_SP1 + 262144;

DI unsigned cvtpk(float lo, float hi) { f32x2 v = {lo, hi}; bf16x2_t r = __builtin_convertvector(v, bf16x2_t); return __builtin_bit_cast(unsigned, r); }
DI float bflo(unsigned u) { return __uint_as_float(u << 16); }
DI float bfhi(unsigned u) { return __uint_as_float(u & 0xffff0000u); }
DI float bf2f(unsigned short u) { return __uint_as_float(((unsigned)u) << 16); }
DI int perm16(int h, int j) { return 8 * (j >> 2) + 4 * h + (j & 3); }
DI float xor32(float v) {
  auto r = __builtin_amdgcn_permlane32_swap(__float_as_uint(v), __float_as_uint(v), false, false);
  return (threadIdx.x & 32) ? __uint_as_float(r[0]) : __uint_as_float(r[1]);
}
DI void glds16(const void* sbase, unsigned voff, unsigned ldsaddr) {
  unsigned keep;
  asm volatile("s_mov_b32 %0, m0\n\ts_mov_b32 m0, %3\n\ts_nop 0\n\tglobal_load_lds_dwordx4 %1, %2\n\ts_mov_b32 m0, %0"
               : "=&s"(keep) : "v"(voff), "s"(sbase), "s"(ldsaddr) : "memory");
}
template <int N> DI void wait_vm_bar() { asm volatile("s_waitcnt vmcnt(%0) lgkmcnt(0)\n\ts_barrier" ::"i"(N) : "memory"); }
DI void bar_lgkm() { asm volatile("s_waitcnt lgkmcnt(0)\n\ts_barrier" ::: "memory"); }
template <int I, int N, class Fn> DI void static_for(Fn&& f) { if constexpr (I < N) { f(std::integral_constant<int, I>{}); static_for<I + 1, N>(f); } }

__global__ __launch_bounds__(256) void prep_kernel(const unsigned short* x, const unsigned short* wq, const unsigned short* wk, const unsigned short* wv,
                                                   const unsigned short* wo, const unsigned short* w1, const unsigned short* w2, char* ws) {
  const int tid = threadIdx.x, lane = tid & 63, wv_ = tid >> 6;
  const int r = lane & 31, h = lane >> 5;
  int b = blockIdx.x;
  if (b < 952) {
    int f = b * 4 + wv_;
    const unsigned short* W; int ldw, nt, ks; char* dst; int ncol;
    if (f < 544) { nt = f / 32; ks = f % 32; dst = ws + OFF_WQKV + (size_t)f * 1024;
      if (nt < 16) { W = wq; ldw = 512; ncol = nt * 32 + r; } else { W = (r < 16) ? wv : wk; ldw = 16; ncol = r & 15; } }
    else if (f < 1056) { int g = f - 544; nt = g / 32; ks = g % 32; dst = ws + OFF_WO + (size_t)g * 1024; W = wo; ldw = 512; ncol = nt * 32 + r; }
    else if (f < 2432) { int g = f - 1056; nt = g / 32; ks = g % 32; dst = ws + OFF_W1 + (size_t)g * 1024; W = w1; ldw = F_; ncol = nt * 32 + r; }
    else { int g = f - 2432; nt = g / KSF; ks = g % KSF; dst = ws + OFF_W2 + (size_t)g * 1024; W = w2; ldw = 512; ncol = nt * 32 + r; }
    unsigned short v[8];
#pragma unroll
    for (int j = 0; j < 8; ++j) v[j] = W[(size_t)(16 * ks + perm16(h, j)) * ldw + ncol];
    u32x4 o;
#pragma unroll
    for (int j = 0; j < 4; ++j) o[j] = (unsigned)v[2 * j] | ((unsigned)v[2 * j + 1] << 16);
    *(u32x4*)(dst + lane * 16) = o;
    return;
  }
  b -= 952;
  if (b < 30) {
    __shared__ float red[256];
    int col = b * 64 + lane; const unsigned short* W; int ldw, c; float* dst;
    if (col < 512) { W = wq; ldw = 512; c = col; dst = (float*)(ws + OFF_CSQ) + col; }
    else if (col < 528) { W = wv; ldw = 16; c = col - 512; dst = (float*)(ws + OFF_CSQ) + col; }
    else if (col < 544) { W = wk; ldw = 16; c = col - 528; dst = (float*)(ws + OFF_CSQ) + col; }
    else { W = w1; ldw = F_; c = col - 544; dst = (float*)(ws + OFF_CS1) + c; }
    float s = 0.f;
#pragma unroll 16
    for (int k = wv_ * 128; k < wv_ * 128 + 128; ++k) s += bf2f(W[(size_t)k * ldw + c]);
    red[tid] = s; __syncthreads();
    if (tid < 64) *dst = red[tid] + red[tid + 64] + red[tid + 128] + red[tid + 192];
    return;
  }
  b -= 30;
  {
    int u = b * 4 + wv_; int tb = u >> 4, nt = u & 15;
    const unsigned short* xr = x + (size_t)(tb * 32 + r) * 512 + nt * 32 + 4 * h;
    u32x2 c0 = *(const u32x2*)(xr), c1 = *(const u32x2*)(xr + 8), c2 = *(const u32x2*)(xr + 16), c3 = *(const u32x2*)(xr + 24);
    u32x4 f0 = {c0[0], c0[1], c1[0], c1[1]}, f1 = {c2[0], c2[1], c3[0], c3[1]};
    char* dst = ws + OFF_X0F + ((size_t)(tb * KSD + 2 * nt) * 1024) + lane * 16;
    *(u32x4*)dst = f0; *(u32x4*)(dst + 1024) = f1;
    float s = 0.f, q = 0.f;
#pragma unroll
    for (int i = 0; i < 4; ++i) { float a = bflo(f0[i]), bb = bfhi(f0[i]), c = bflo(f1[i]), d = bfhi(f1[i]); s += a + bb + c + d; q += a * a + bb * bb + c * c + d * d; }
    s += xor32(s); q += xor32(q);
    if (h == 0) { f32x2 o = {s, q}; *(f32x2*)(ws + OFF_SP0 + ((size_t)nt * S_ + tb * 32 + r) * 8) = o; }
  }
}

enum { EPI_QKV = 0, EPI_RES = 1, EPI_GELU = 2, EPI_OUT = 3 };

DI void ln_stats(const char* sp, int tb, int lane, float& mean, float& rstd) {
  const int r = lane & 31, h = lane >> 5;
  const f32x2* p = (const f32x2*)sp + (size_t)(8 * h) * S_ + tb * 32 + r;
  float s = 0.f, q = 0.f;
#pragma unroll
  for (int i = 0; i < 8; ++i) { f32x2 v = p[(size_t)i * S_]; s += v[0]; q += v[1]; }
  s += xor32(s); q += xor32(q);
  mean = s * (1.f / 512.f);
  float var = fmaxf(q * (1.f / 512.f) - mean * mean, 0.f);
  rstd = __builtin_amdgcn_rsqf(var + 1e-5f);
}
DI void load_featvec(const float* base, int h, float* o) {
#pragma unroll
  for (int g = 0; g < 4; ++g) { f32x4 v = *(const f32x4*)(base + 8 * g + 4 * h); o[4 * g] = v[0]; o[4 * g + 1] = v[1]; o[4 * g + 2] = v[2]; o[4 * g + 3] = v[3]; }
}
DI void load_featvec_bf(const unsigned short* base, int h, float* o) {
#pragma unroll
  for (int g = 0; g < 4; ++g) { u32x2 v = *(const u32x2*)(base + 8 * g + 4 * h); o[4 * g] = bflo(v[0]); o[4 * g + 1] = bfhi(v[0]); o[4 * g + 2] = bflo(v[1]); o[4 * g + 3] = bfhi(v[1]); }
}
DI void store_frags(char* dst, const float* v, int lane) {
  u32x4 f0 = {cvtpk(v[0], v[1]), cvtpk(v[2], v[3]), cvtpk(v[4], v[5]), cvtpk(v[6], v[7])};
  u32x4 f1 = {cvtpk(v[8], v[9]), cvtpk(v[10], v[11]), cvtpk(v[12], v[13]), cvtpk(v[14], v[15])};
  *(u32x4*)(dst + lane * 16) = f0; *(u32x4*)(dst + 1024 + lane * 16) = f1;
}

template <int MT, int NT, int TM, int TN, int KS, int CH, int SL, int EPI, bool KV>
DI void gemm_wg(char* lds, const char* WF, const char* XF, int nt0, int ntmax, int tb0, int kvw,
                const char* sp, const float* cs, const unsigned short* bias, const char* resF, char* outF, char* ws, unsigned short* outRM, char* spOut) {
  constexpr int NF = MT + NT + (KV ? 1 : 0);
  constexpr int CHB = CH * NF * 1024;
  constexpr int NC = (KS + CH - 1) / CH;
  constexpr int PER = CH * NF / 4;
  static_assert((CH * NF) % 4 == 0, "");
  const int tid = threadIdx.x, lane = tid & 63;
  const int w = __builtin_amdgcn_readfirstlane(tid >> 6);
  const unsigned lds0 = (unsigned)(uintptr_t)lds;
  if (w >= 4) {
    const int lw = w - 4; const unsigned voff = lane * 16;
    auto issue = [&](int c) {
#pragma unroll
      for (int p = 0; p < PER; ++p) {
        int idx = p * 4 + lw; int i = idx / NF, f = idx % NF;
        int ks = c * CH + i; if (ks > KS - 1) ks = KS - 1;
        const char* src;
        if (f < MT) src = XF + ((size_t)(tb0 + f) * KS + ks) * 1024;
        else if (f < MT + NT) { int nt = nt0 + (f - MT); if (nt > ntmax) nt = ntmax; src = WF + ((size_t)nt * KS + ks) * 1024; }
        else src = WF + ((size_t)16 * KS + ks) * 1024;
        glds16(src, voff, lds0 + (c % SL) * CHB + idx * 1024);
      }
    };
#pragma unroll
    for (int c = 0; c < SL - 1 && c < NC; ++c) issue(c);
    static_for<0, NC>([&](auto ci) {
      constexpr int c = decltype(ci)::value;
      constexpr int infl = ((c + SL - 2 < NC - 1) ? (c + SL - 2) : (NC - 1)) - c;
      wait_vm_bar<infl * PER>();
      if constexpr (c + SL - 1 < NC) issue(c + SL - 1);
    });
    return;
  }
  const int r = lane & 31, h = lane >> 5;
  constexpr int WN = NT / TN;
  const int tl0 = (w / WN) * TM, nl0 = (w % WN) * TN;
  f32x16 acc[TM][TN];
#pragma unroll
  for (int a = 0; a < TM; ++a)
#pragma unroll
    for (int b = 0; b < TN; ++b)
#pragma unroll
      for (int i = 0; i < 16; ++i) acc[a][b][i] = 0.f;
  f32x16 kvacc;
#pragma unroll
  for (int i = 0; i < 16; ++i) kvacc[i] = 0.f;
  float mean = 0.f, rstd = 0.f;
  if constexpr (EPI == EPI_QKV || EPI == EPI_GELU) ln_stats(sp, tb0 + tl0, lane, mean, rstd);
  u32x4 res[2];
  if constexpr (EPI == EPI_RES || EPI == EPI_OUT) {
    const char* rp = resF + ((size_t)(tb0 + tl0) * KSD + 2 * (nt0 + nl0)) * 1024 + lane * 16;
    res[0] = *(const u32x4*)rp; res[1] = *(const u32x4*)(rp + 1024);
  }
  const bool kvme = KV && (tl0 == kvw);
  const LAS char* lbase = (const LAS char*)lds + lane * 16;
#pragma unroll
  for (int c = 0; c < NC; ++c) {
    bar_lgkm();
    const LAS char* cb = lbase + (c % SL) * CHB;
#pragma unroll
    for (int i = 0; i < CH; ++i) {
      if (c * CH + i < KS) {
        bf16x8 bfr[TM], afr[TN];
#pragma unroll
        for (int a = 0; a < TM; ++a) bfr[a] = *(const LAS bf16x8*)(cb + (i * NF + tl0 + a) * 1024);
#pragma unroll
        for (int b = 0; b < TN; ++b) afr[b] = *(const LAS bf16x8*)(cb + (i * NF + MT + nl0 + b) * 1024);
#pragma unroll
        for (int a = 0; a < TM; ++a)
#pragma unroll
          for (int b = 0; b < TN; ++b) acc[a][b] = MFMA(afr[b], bfr[a], acc[a][b]);
        if constexpr (KV) {
          if (kvme) { bf16x8 kvf = *(const LAS bf16x8*)(cb + (i * NF + MT + NT) * 1024);
            if (nl0 == 0) kvacc = MFMA(kvf, bfr[0], kvacc); else kvacc = MFMA(bfr[0], kvf, kvacc); }
        }
      }
    }
  }
  const int tb = tb0 + tl0;
  if constexpr (EPI == EPI_QKV) {
    const float QS = 0.25f * 1.4426950408889634f;
    const int nt = nt0 + nl0; float csv[16], o[16];
    load_featvec(cs + nt * 32, h, csv);
    const float a = rstd * QS, b2 = -mean * rstd * QS;
#pragma unroll
    for (int i = 0; i < 16; ++i) o[i] = acc[0][0][i] * a + b2 * csv[i];
    store_frags(outF + ((size_t)tb * KSD + 2 * nt) * 1024, o, lane);
    if constexpr (KV) {
      if (kvme) {
        if (nl0 == 0) {
          load_featvec(cs + 512, h, csv);
          const float b3 = -mean * rstd;
          u32x4 f = {cvtpk(kvacc[8] * rstd + b3 * csv[8], kvacc[9] * rstd + b3 * csv[9]), cvtpk(kvacc[10] * rstd + b3 * csv[10], kvacc[11] * rstd + b3 * csv[11]),
                     cvtpk(kvacc[12] * rstd + b3 * csv[12], kvacc[13] * rstd + b3 * csv[13]), cvtpk(kvacc[14] * rstd + b3 * csv[14], kvacc[15] * rstd + b3 * csv[15])};
          *(u32x4*)(ws + OFF_KF + (size_t)tb * 1024 + lane * 16) = f;
        } else {
          const float csd = cs[512 + (r & 15)];
          float o2[16];
#pragma unroll
          for (int i = 0; i < 16; ++i) { int t = (i & 3) + 8 * (i >> 2) + 4 * h; float m = __shfl(mean, t), rs = __shfl(rstd, t); o2[i] = (kvacc[i] - m * csd) * rs; }
          if (r < 16) {
            u32x4 f0 = {cvtpk(o2[0], o2[1]), cvtpk(o2[2], o2[3]), cvtpk(o2[4], o2[5]), cvtpk(o2[6], o2[7])};
            u32x4 f1 = {cvtpk(o2[8], o2[9]), cvtpk(o2[10], o2[11]), cvtpk(o2[12], o2[13]), cvtpk(o2[14], o2[15])};
            char* d = ws + OFF_VF + (size_t)tb * 1024 + (h * 16 + r) * 16;
            *(u32x4*)d = f0; *(u32x4*)(d + 512) = f1;
          }
        }
      }
    }
  } else if constexpr (EPI == EPI_GELU) {
    const float C1 = -2.f * 0.7978845608028654f * 1.4426950408889634f, C2 = C1 * 0.044715f;
    const float b2 = -mean * rstd;
#pragma unroll
    for (int b = 0; b < TN; ++b) {
      const int nt = nt0 + nl0 + b;
      if (nt <= ntmax) {
        float csv[16], bv[16], o[16];
        load_featvec(cs + nt * 32, h, csv); load_featvec_bf(bias + nt * 32, h, bv);
#pragma unroll
        for (int i = 0; i < 16; ++i) {
          float x = acc[0][b][i] * rstd + (b2 * csv[i] + bv[i]);
          float z = x * (C1 + C2 * x * x);
          o[i] = x * __builtin_amdgcn_rcpf(1.f + __builtin_amdgcn_exp2f(z));
        }
        store_frags(outF + ((size_t)tb * KSF + 2 * nt) * 1024, o, lane);
      }
    }
  } else {
    const int nt = nt0 + nl0; float o[16], bv[16];
    if (bias) load_featvec_bf(bias + nt * 32, h, bv); else { for (int i = 0; i < 16; ++i) bv[i] = 0.f; }
#pragma unroll
    for (int i = 0; i < 4; ++i) {
      o[2 * i] = acc[0][0][2 * i] + bv[2 * i] + bflo(res[0][i]); o[2 * i + 1] = acc[0][0][2 * i + 1] + bv[2 * i + 1] + bfhi(res[0][i]);
      o[8 + 2 * i] = acc[0][0][8 + 2 * i] + bv[8 + 2 * i] + bflo(res[1][i]); o[9 + 2 * i] = acc[0][0][9 + 2 * i] + bv[9 + 2 * i] + bfhi(res[1][i]);
    }
    u32x4 f0 = {cvtpk(o[0], o[1]), cvtpk(o[2], o[3]), cvtpk(o[4], o[5]), cvtpk(o[6], o[7])};
    u32x4 f1 = {cvtpk(o[8], o[9]), cvtpk(o[10], o[11]), cvtpk(o[12], o[13]), cvtpk(o[14], o[15])};
    if constexpr (EPI == EPI_RES) {
      char* d = outF + ((size_t)tb * KSD + 2 * nt) * 1024 + lane * 16;
      *(u32x4*)d = f0; *(u32x4*)(d + 1024) = f1;
      float s = 0.f, q = 0.f;
#pragma unroll
      for (int i = 0; i < 4; ++i) { float a = bflo(f0[i]), bb = bfhi(f0[i]), c = bflo(f1[i]), d2 = bfhi(f1[i]); s += a + bb + c + d2; q += a * a + bb * bb + c * c + d2 * d2; }
      s += xor32(s); q += xor32(q);
      if (h == 0) { f32x2 st = {s, q}; *(f32x2*)(spOut + ((size_t)nt * S_ + tb * 32 + r) * 8) = st; }
    } else {
      unsigned short* d = outRM + (size_t)(tb * 32 + r) * 512 + nt * 32 + 4 * h;
      *(u32x2*)(d) = u32x2{f0[0], f0[1]}; *(u32x2*)(d + 8) = u32x2{f0[2], f0[3]};
      *(u32x2*)(d + 16) = u32x2{f1[0], f1[1]}; *(u32x2*)(d + 24) = u32x2{f1[2], f1[3]};
    }
  }
}

__global__ __launch_bounds__(512, 2) void qkv_kernel(const char* XF, const char* sp, char* ws) {
  extern __shared__ __attribute__((aligned(16))) char lds[];
  const int b = blockIdx.x, x = b & 7, y = b >> 3; const int tp = 4 * x + (y >> 3), cg = y & 7;
  const char* WF = ws + OFF_WQKV; const float* cs = (const float*)(ws + OFF_CSQ);
  if (cg < 2) gemm_wg<2, 2, 1, 1, KSD, 4, 6, EPI_QKV, true>(lds, WF, XF, cg * 2, 15, tp * 2, cg, sp, cs, nullptr, nullptr, ws + OFF_QF, ws, nullptr, nullptr);
  else gemm_wg<2, 2, 1, 1, KSD, 4, 6, EPI_QKV, false>(lds, WF, XF, cg * 2, 15, tp * 2, 0, sp, cs, nullptr, nullptr, ws + OFF_QF, ws, nullptr, nullptr);
}
__global__ __launch_bounds__(512, 2) void oproj_kernel(const char* resF, char* outF, char* spOut, char* ws) {
  extern __shared__ __attribute__((aligned(16))) char lds[];
  const int b = blockIdx.x, x = b & 7, y = b >> 3; const int tp = 4 * x + (y >> 3), cg = y & 7;
  gemm_wg<2, 2, 1, 1, KSD, 4, 8, EPI_RES, false>(lds, ws + OFF_WO, ws + OFF_OF, cg * 2, 15, tp * 2, 0, nullptr, nullptr, nullptr, resF, outF, ws, nullptr, spOut);
}
__global__ __launch_bounds__(512, 2) void ffn1_kernel(const char* XF, const char* sp, const unsigned short* b1, char* ws) {
  extern __shared__ __attribute__((aligned(16))) char lds[];
  const int b = blockIdx.x, x = b & 7, y = b >> 3; const int tq = 2 * x + y / 15, cg = y % 15;
  gemm_wg<4, 3, 1, 3, KSD, 4, 5, EPI_GELU, false>(lds, ws + OFF_W1, XF, cg * 3, NTF - 1, tq * 4, 0, sp, (const float*)(ws + OFF_CS1), b1, nullptr, ws + OFF_HF, ws, nullptr, nullptr);
}
template <bool LAST>
__global__ __launch_bounds__(512, 2) void ffn2_kernel(const char* resF, const unsigned short* b2, char* outF, char* spOut, unsigned short* outRM, char* ws) {
  extern __shared__ __attribute__((aligned(16))) char lds[];
  const int b = blockIdx.x, x = b & 7, y = b >> 3; const int tp = 4 * x + (y >> 3), cg = y & 7;
  gemm_wg<2, 2, 1, 1, KSF, 4, 8, LAST ? EPI_OUT : EPI_RES, false>(lds, ws + OFF_W2, ws + OFF_HF, cg * 2, 15, tp * 2, 0, nullptr, nullptr, b2, resF, outF, ws, outRM, spOut);
}

constexpr int AL_K = 0, AL_V = 65536, AL_ONES = 131072, AL_Q = AL_ONES + 4096, AL_BYTES = AL_Q + 8192;

DI void attn_pass(const LAS char* lds, int lane, bf16x8 q, const f32x16& cinit, f32x16& o, bool first) {
  const LAS char* kp = lds + AL_K + lane * 16;
  const bool vl = (lane & 16) == 0;
  const int slot = ((lane >> 5) * 16 + (lane & 15)) * 16;
  const LAS char* vp = lds + (vl ? AL_V : AL_ONES) + slot;
  const int vinc = vl ? 4096 : 0;
#pragma unroll
  for (int i = 0; i < 16; ++i) o[i] = 0.f;
  for (int c = 0; c < 4; ++c) {
    if (first) {
      if (c == 0) wait_vm_bar<12>(); else if (c == 1) wait_vm_bar<8>(); else if (c == 2) wait_vm_bar<4>(); else wait_vm_bar<0>();
      if (c == 0) q = *(const LAS bf16x8*)(lds + AL_Q + (threadIdx.x >> 6) * 1024 + lane * 16);
    }
    for (int it = 0; it < 4; ++it) {
#pragma unroll
      for (int u = 0; u < 4; ++u) {
        bf16x8 kf = *(const LAS bf16x8*)(kp + u * 1024);
        bf16x8 v0 = *(const LAS bf16x8*)(vp + u * 1024), v1 = *(const LAS bf16x8*)(vp + u * 1024 + 512);
        f32x16 s = MFMA(kf, q, cinit);
        u32x4 p0, p1;
#pragma unroll
        for (int i = 0; i < 4; ++i) {
          p0[i] = cvtpk(__builtin_amdgcn_exp2f(s[2 * i]), __builtin_amdgcn_exp2f(s[2 * i + 1]));
          p1[i] = cvtpk(__builtin_amdgcn_exp2f(s[8 + 2 * i]), __builtin_amdgcn_exp2f(s[9 + 2 * i]));
        }
        o = MFMA(v0, __builtin_bit_cast(bf16x8, p0), o);
        o = MFMA(v1, __builtin_bit_cast(bf16x8, p1), o);
      }
      kp += 4096; vp += vinc;
    }
  }
}

__global__ __launch_bounds__(512, 2) void attn_kernel(char* ws) {
  extern __shared__ __attribute__((aligned(16))) char lds[];
  const int tid = threadIdx.x, lane = tid & 63; const int w = __builtin_amdgcn_readfirstlane(tid >> 6);
  const int b = blockIdx.x, x = b & 7, y = b >> 3; const int tb = 8 * x + (y >> 2), head = 8 * (y & 3) + w;
  const unsigned lds0 = (unsigned)(uintptr_t)lds; const unsigned voff = lane * 16;
  glds16(ws + OFF_QF + ((size_t)tb * KSD + head) * 1024, voff, lds0 + AL_Q + w * 1024);
#pragma unroll
  for (int c = 0; c < 4; ++c) {
#pragma unroll
    for (int p = 0; p < 2; ++p) { int t = c * 16 + p * 8 + w;
      glds16(ws + OFF_KF + (size_t)t * 1024, voff, lds0 + AL_K + t * 1024);
      glds16(ws + OFF_VF + (size_t)t * 1024, voff, lds0 + AL_V + t * 1024); }
  }
  *(LAS u32x2*)((LAS char*)lds + AL_ONES + tid * 8) = u32x2{0x3f803f80u, 0x3f803f80u};
  const LAS char* l3 = (const LAS char*)lds;
  f32x16 cinit, o;
#pragma unroll
  for (int i = 0; i < 16; ++i) cinit[i] = 0.f;
  bf16x8 q = {};
  attn_pass(l3, lane, q, cinit, o, true);
  q = *(const LAS bf16x8*)(l3 + AL_Q + w * 1024 + lane * 16);
  float l = o[8];
  bool ok = (l >= 0x1p-60f) && (l <= 0x1p100f);
#pragma unroll
  for (int i = 0; i < 8; ++i) ok = ok && (__builtin_fabsf(o[i]) <= 3.0e38f);
  if (!__all(ok)) {
    float m = -INFINITY;
    const LAS char* kp = l3 + AL_K + lane * 16;
    f32x16 z;
#pragma unroll
    for (int i = 0; i < 16; ++i) z[i] = 0.f;
    for (int t = 0; t < 64; ++t) {
      f32x16 s = MFMA(*(const LAS bf16x8*)(kp + t * 1024), q, z);
#pragma unroll
      for (int i = 0; i < 16; ++i) m = fmaxf(m, s[i]);
    }
    m = fmaxf(m, xor32(m));
#pragma unroll
    for (int i = 0; i < 16; ++i) cinit[i] = -m;
    attn_pass(l3, lane, q, cinit, o, false);
    l = o[8];
  }
  const float rl = 1.f / l;
  u32x4 f = {cvtpk(o[0] * rl, o[1] * rl), cvtpk(o[2] * rl, o[3] * rl), cvtpk(o[4] * rl, o[5] * rl), cvtpk(o[6] * rl, o[7] * rl)};
  *(u32x4*)(ws + OFF_OF + ((size_t)tb * KSD + head) * 1024 + lane * 16) = f;
}

extern "C" void kernel_launch(void* const* d_in, const int* in_sizes, int n_in, void* d_out, int out_size, void* d_ws, size_t ws_size, hipStream_t stream) {
  const unsigned short* x = (const unsigned short*)d_in[0];
  const unsigned short* wq = (const unsigned short*)d_in[1];
  const unsigned short* wk = (const unsigned short*)d_in[2];
  const unsigned short* wv = (const unsigned short*)d_in[3];
  const unsigned short* wo = (const unsigned short*)d_in[4];
  const unsigned short* w1 = (const unsigned short*)d_in[6];
  const unsigned short* b1 = (const unsigned short*)d_in[7];
  const unsigned short* w2 = (const unsigned short*)d_in[8];
  const unsigned short* b2 = (const unsigned short*)d_in[9];
  char* ws = (char*)d_ws;
  constexpr int LDS_QKV = 6 * 4 * 5 * 1024, LDS_OP = 8 * 4 * 4 * 1024, LDS_F1 = 5 * 4 * 7 * 1024, LDS_F2 = 8 * 4 * 4 * 1024;
  hipFuncSetAttribute((const void*)qkv_kernel, hipFuncAttributeMaxDynamicSharedMemorySize, LDS_QKV);
  hipFuncSetAttribute((const void*)oproj_kernel, hipFuncAttributeMaxDynamicSharedMemorySize, LDS_OP);
  hipFuncSetAttribute((const void*)ffn1_kernel, hipFuncAttributeMaxDynamicSharedMemorySize, LDS_F1);
  hipFuncSetAttribute((const void*)ffn2_kernel<false>, hipFuncAttributeMaxDynamicSharedMemorySize, LDS_F2);
  hipFuncSetAttribute((const void*)ffn2_kernel<true>, hipFuncAttributeMaxDynamicSharedMemorySize, LDS_F2);
  hipFuncSetAttribute((const void*)attn_kernel, hipFuncAttributeMaxDynamicSharedMemorySize, AL_BYTES);
  prep_kernel<<<952 + 30 + 256, 256, 0, stream>>>(x, wq, wk, wv, wo, w1, w2, ws);
  qkv_kernel<<<256, 512, LDS_QKV, stream>>>(ws + OFF_X0F, ws + OFF_SP0, ws);
  attn_kernel<<<256, 512, AL_BYTES, stream>>>(ws);
  oproj_kernel<<<256, 512, LDS_OP, stream>>>(ws + OFF_X0F, ws + OFF_X1F, ws + OFF_SP1, ws);
  ffn1_kernel<<<240, 512, LDS_F1, stream>>>(ws + OFF_X1F, ws + OFF_SP1, b1, ws);
  ffn2_kernel<false><<<256, 512, LDS_F2, stream>>>(ws + OFF_X1F, b2, ws + OFF_X2F, ws + OFF_SP2, nullptr, ws);
  qkv_kernel<<<256, 512, LDS_QKV, stream>>>(ws + OFF_X2F, ws + OFF_SP2, ws);
  attn_kernel<<<256, 512, AL_BYTES, stream>>>(ws);
  oproj_kernel<<<256, 512, LDS_OP, stream>>>(ws + OFF_X2F, ws + OFF_X1F, ws + OFF_SP1, ws);
  ffn1_kernel<<<240, 512, LDS_F1, stream>>>(ws + OFF_X1F, ws + OFF_SP1, b1, ws);
  ffn2_kernel<true><<<256, 512, LDS_F2, stream>>>(ws + OFF_X1F, b2, nullptr, nullptr, (unsigned short*)d_out, ws);
}
```

```cpp
#include <hip/hip_runtime.h>
#include <hip/hip_bf16.h>
#include <stdint.h>

typedef __attribute__((ext_vector_type(8))) short bf16x8;
typedef __attribute__((ext_vector_type(16))) float f32x16;
typedef __attribute__((ext_vector_type(4))) unsigned u32x4;
typedef __attribute__((ext_vector_type(2))) unsigned u32x2;
typedef __attribute__((ext_vector_type(2))) float f32x2;
typedef __attribute__((ext_vector_type(4))) float f32x4;
typedef __bf16 bf16x2_t __attribute__((ext_vector_type(2)));
#define LAS __attribute__((address_space(3)))
#define GAS __attribute__((address_space(1)))
#define DI __device__ __forceinline__
#define MFMA(a, b, c) __builtin_amdgcn_mfma_f32_32x32x16_bf16((a), (b), (c), 0, 0, 0)
#define RLX_AGENT __ATOMIC_RELAXED, __HIP_MEMORY_SCOPE_AGENT

constexpr int S_ = 2048, F_ = 1376;
constexpr int KSD = 32, KSF = 86;

constexpr size_t MB = 1024 * 1024;
constexpr unsigned OFF_QF = 0;
constexpr unsigned OFF_X1F = 2 * MB;
constexpr unsigned OFF_X2F = 6 * MB;
constexpr unsigned OFF_OF = 8 * MB;
constexpr unsigned OFF_HF = 12 * MB;
constexpr unsigned OFF_KF = 24 * MB;
constexpr unsigned OFF_VF = OFF_KF + 131072;
constexpr unsigned OFF_WQKV = 25 * MB;
constexpr unsigned OFF_WO = 26 * MB;
constexpr unsigned OFF_W1 = 27 * MB;
constexpr unsigned OFF_W2 = 29 * MB;
constexpr unsigned OFF_CSQ = 31 * MB;
constexpr unsigned OFF_CS1 = OFF_CSQ + 4096;
constexpr unsigned OFF_SP = 33 * MB;
constexpr unsigned OFF_SYNC = 32 * MB;
constexpr unsigned OFF_KVFLAG = OFF_SYNC + 65536;
constexpr unsigned OFF_WRDY = OFF_KVFLAG + 2048;
constexpr unsigned SYNC_BYTES = 65536 + 2048 + 8192;
constexpr unsigned WS_USED = 36 * MB;

DI unsigned cvtpk(float lo, float hi) { f32x2 v = {lo, hi}; bf16x2_t r = __builtin_convertvector(v, bf16x2_t); return __builtin_bit_cast(unsigned, r); }
DI float bflo(unsigned u) { return __uint_as_float(u << 16); }
DI float bfhi(unsigned u) { return __uint_as_float(u & 0xffff0000u); }
DI float bf2f(unsigned short u) { return __uint_as_float(((unsigned)u) << 16); }
DI float add32(float v) { auto r = __builtin_amdgcn_permlane32_swap(__float_as_uint(v), __float_as_uint(v), false, false); return __uint_as_float(r[0]) + __uint_as_float(r[1]); }
DI float max32(float v) { auto r = __builtin_amdgcn_permlane32_swap(__float_as_uint(v), __float_as_uint(v), false, false); return fmaxf(__uint_as_float(r[0]), __uint_as_float(r[1])); }
DI float dot2(unsigned a, unsigned b, float c) { return __builtin_amdgcn_fdot2_f32_bf16(__builtin_bit_cast(bf16x2_t, a), __builtin_bit_cast(bf16x2_t, b), c, false); }
template <bool SC1> DI void glds4(const void* sbase, unsigned voff, unsigned ldsaddr) {
  if constexpr (SC1) asm volatile("s_mov_b32 m0, %2\n\ts_nop 0\n\tglobal_load_lds_dwordx4 %0, %1 sc1\n\tglobal_load_lds_dwordx4 %0, %1 offset:1024 sc1\n\t"
                                  "global_load_lds_dwordx4 %0, %1 offset:2048 sc1\n\tglobal_load_lds_dwordx4 %0, %1 offset:3072 sc1" ::"v"(voff), "s"(sbase), "s"(ldsaddr) : "memory");
  else asm volatile("s_mov_b32 m0, %2\n\ts_nop 0\n\tglobal_load_lds_dwordx4 %0, %1\n\tglobal_load_lds_dwordx4 %0, %1 offset:1024\n\t"
                    "global_load_lds_dwordx4 %0, %1 offset:2048\n\tglobal_load_lds_dwordx4 %0, %1 offset:3072" ::"v"(voff), "s"(sbase), "s"(ldsaddr) : "memory");
}
DI void glds1(const void* sbase, unsigned voff, unsigned ldsaddr) { asm volatile("s_mov_b32 m0, %2\n\ts_nop 0\n\tglobal_load_lds_dwordx4 %0, %1" ::"v"(voff), "s"(sbase), "s"(ldsaddr) : "memory"); }
DI void glds1v(const void* vaddr, unsigned ldsaddr) { asm volatile("s_mov_b32 m0, %1\n\ts_nop 0\n\tglobal_load_lds_dwordx4 %0, off" ::"v"(vaddr), "s"(ldsaddr) : "memory"); }
template <int N> DI void wait_vm_bar() { asm volatile("s_waitcnt vmcnt(%0) lgkmcnt(0)\n\ts_barrier" ::"i"(N) : "memory"); }
DI void bar_lgkm() { asm volatile("s_waitcnt lgkmcnt(0)\n\ts_barrier" ::: "memory"); }
DI void drain_bar() { asm volatile("s_waitcnt vmcnt(0) lgkmcnt(0)\n\ts_barrier" ::: "memory"); }
template <int I, int N, class Fn> DI void static_for(Fn&& f) { if constexpr (I < N) { f(std::integral_constant<int, I>{}); static_for<I + 1, N>(f); } }
template <int V> using IC = std::integral_constant<int, V>;
constexpr int imin(int a, int b) { return a < b ? a : b; }
template <class T> DI T gld(const void* p) { return *(const GAS T*)p; }
typedef __amdgpu_buffer_rsrc_t rsrc_t;
DI void st_sc1(rsrc_t rs, unsigned off, u32x4 v) { __builtin_amdgcn_raw_buffer_store_b128(v, rs, off, 0, 16); }
constexpr unsigned SPIN_CAP = 1u << 16;

typedef short s16x4_t __attribute__((ext_vector_type(4)));
DI u32x4 lds_block_to_frag(const LAS char* blk, int lane) {
  const int g16 = lane >> 4, q = (lane & 15) >> 2, p = lane & 3, h = g16 >> 1;
  const LAS char* a = blk + (4 * h + q) * 64 + (16 * (g16 & 1) + 4 * p) * 2;
  const s16x4_t lo = __builtin_amdgcn_ds_read_tr16_b64_v4i16((LAS s16x4_t*)a), hi = __builtin_amdgcn_ds_read_tr16_b64_v4i16((LAS s16x4_t*)(a + 8 * 64));
  const u32x2 l2 = __builtin_bit_cast(u32x2, lo), h2 = __builtin_bit_cast(u32x2, hi);
  return u32x4{l2[0], l2[1], h2[0], h2[1]};
}
#define ATTN_ASM \
    "v_mov_b32 v112, %[cst]\n\t" \
    "v_mov_b32 v113, %[cst]\n\t" \
    "v_mov_b32 v114, %[cst]\n\t" \
    "v_mov_b32 v115, %[cst]\n\t" \
    "v_mov_b32 v116, %[cst]\n\t" \
    "v_mov_b32 v117, %[cst]\n\t" \
    "v_mov_b32 v118, %[cst]\n\t" \
    "v_mov_b32 v119, %[cst]\n\t" \
    "v_mov_b32 v120, %[cst]\n\t" \
    "v_mov_b32 v121, %[cst]\n\t" \
    "v_mov_b32 v122, %[cst]\n\t" \
    "v_mov_b32 v123, %[cst]\n\t" \
    "v_mov_b32 v124, %[cst]\n\t" \
    "v_mov_b32 v125, %[cst]\n\t" \
    "v_mov_b32 v126, %[cst]\n\t" \
    "v_mov_b32 v127, %[cst]\n\t" \
    "s_mov_b32 %[pc], 0\n\t" \
    "s_waitcnt lgkmcnt(0)\n\t" \
    "s_barrier\n\t" \
    "ds_read_b128 %[q], %[qa]\n\t" \
    "ds_read_b128 %[q2], %[qb]\n\t" \
    "ds_read_b128 v[104:107], %[ka]\n\t" \
    "ds_read_b128 v[108:111], %[ka] offset:1024\n\t" \
    "s_mov_b64 exec, %[vm]\n\t" \
    "ds_read_b128 v[112:115], %[va]\n\t" \
    "ds_read_b128 v[116:119], %[va] offset:512\n\t" \
    "s_mov_b64 exec, -1\n\t" \
    "s_waitcnt lgkmcnt(0)\n\t" \
    "v_mfma_f32_32x32x16_bf16 v[64:79], v[104:107], %[q], 0\n\t" \
    "s_mov_b32 %[it], 0\n\t" \
    "s_nop 10\n\t" \
    "v_exp_f32 v64, v64\n\t" \
    "v_exp_f32 v65, v65\n\t" \
    "1:\n\t" \
    "s_cmp_eq_u32 %[it], 1\n\t" \
    "s_cbranch_scc0 2f\n\t" \
    "s_barrier\n\t" \
    "2:\n\t" \
    "s_add_u32 %[pc], %[pc], 1\n\t" \
    "v_mov_b32 %[pt], %[pc]\n\t" \
    "ds_write_b32 %[pa], %[pt]\n\t" \
    "ds_read_b32 %[pu], %[pb]\n\t" \
    "v_mfma_f32_32x32x16_bf16 v[80:95], v[104:107], %[q2], 0\n\t" \
    "v_exp_f32 v66, v66\n\t" \
    "v_exp_f32 v67, v67\n\t" \
    "v_exp_f32 v68, v68\n\t" \
    "v_exp_f32 v69, v69\n\t" \
    "v_cvt_pk_bf16_f32 v96, v64, v65\n\t" \
    "v_cvt_pk_bf16_f32 v97, v66, v67\n\t" \
    "v_exp_f32 v70, v70\n\t" \
    "v_exp_f32 v71, v71\n\t" \
    "v_exp_f32 v72, v72\n\t" \
    "v_exp_f32 v73, v73\n\t" \
    "v_cvt_pk_bf16_f32 v98, v68, v69\n\t" \
    "v_cvt_pk_bf16_f32 v99, v70, v71\n\t" \
    "v_exp_f32 v74, v74\n\t" \
    "v_exp_f32 v75, v75\n\t" \
    "s_waitcnt lgkmcnt(3)\n\t" \
    "v_mfma_f32_32x32x16_bf16 %[o], v[112:115], v[96:99], %[o]\n\t" \
    "s_mov_b64 exec, %[vm]\n\t" \
    "ds_read_b128 v[120:123], %[va] offset:1024\n\t" \
    "ds_read_b128 v[124:127], %[va] offset:1536\n\t" \
    "s_mov_b64 exec, -1\n\t" \
    "v_exp_f32 v76, v76\n\t" \
    "v_exp_f32 v77, v77\n\t" \
    "v_cvt_pk_bf16_f32 v100, v72, v73\n\t" \
    "v_cvt_pk_bf16_f32 v101, v74, v75\n\t" \
    "v_exp_f32 v78, v78\n\t" \
    "v_exp_f32 v79, v79\n\t" \
    "s_nop 0\n\t" \
    "v_cvt_pk_bf16_f32 v102, v76, v77\n\t" \
    "v_cvt_pk_bf16_f32 v103, v78, v79\n\t" \
    "v_exp_f32 v80, v80\n\t" \
    "v_exp_f32 v81, v81\n\t" \
    "v_mfma_f32_32x32x16_bf16 %[o], v[116:119], v[100:103], %[o]\n\t" \
    "s_waitcnt lgkmcnt(2)\n\t" \
    "v_readfirstlane_b32 %[ps], %[pu]\n\t" \
    "s_nop 0\n\t" \
    "s_cmp_gt_u32 %[pc], %[ps]\n\t" \
    "s_cbranch_scc1 8f\n\t" \
    "s_setprio 1\n\t" \
    "s_branch 9f\n\t" \
    "8:\n\t" \
    "s_setprio 0\n\t" \
    "9:\n\t" \
    "v_mfma_f32_32x32x16_bf16 v[64:79], v[108:111], %[q], 0\n\t" \
    "v_exp_f32 v82, v82\n\t" \
    "v_exp_f32 v83, v83\n\t" \
    "v_exp_f32 v84, v84\n\t" \
    "v_exp_f32 v85, v85\n\t" \
    "v_cvt_pk_bf16_f32 v96, v80, v81\n\t" \
    "v_cvt_pk_bf16_f32 v97, v82, v83\n\t" \
    "v_exp_f32 v86, v86\n\t" \
    "v_exp_f32 v87, v87\n\t" \
    "v_exp_f32 v88, v88\n\t" \
    "v_exp_f32 v89, v89\n\t" \
    "v_cvt_pk_bf16_f32 v98, v84, v85\n\t" \
    "v_cvt_pk_bf16_f32 v99, v86, v87\n\t" \
    "v_exp_f32 v90, v90\n\t" \
    "v_exp_f32 v91, v91\n\t" \
    "v_mfma_f32_32x32x16_bf16 %[o2], v[112:115], v[96:99], %[o2]\n\t" \
    "ds_read_b128 v[104:107], %[ka] offset:2048\n\t" \
    "v_exp_f32 v92, v92\n\t" \
    "v_exp_f32 v93, v93\n\t" \
    "v_cvt_pk_bf16_f32 v100, v88, v89\n\t" \
    "v_cvt_pk_bf16_f32 v101, v90, v91\n\t" \
    "v_exp_f32 v94, v94\n\t" \
    "v_exp_f32 v95, v95\n\t" \
    "s_nop 0\n\t" \
    "v_cvt_pk_bf16_f32 v102, v92, v93\n\t" \
    "v_cvt_pk_bf16_f32 v103, v94, v95\n\t" \
    "v_exp_f32 v64, v64\n\t" \
    "v_exp_f32 v65, v65\n\t" \
    "v_mfma_f32_32x32x16_bf16 %[o2], v[116:119], v[100:103], %[o2]\n\t" \
    "v_mfma_f32_32x32x16_bf16 v[80:95], v[108:111], %[q2], 0\n\t" \
    "v_exp_f32 v66, v66\n\t" \
    "v_exp_f32 v67, v67\n\t" \
    "v_exp_f32 v68, v68\n\t" \
    "v_exp_f32 v69, v69\n\t" \
    "v_cvt_pk_bf16_f32 v96, v64, v65\n\t" \
    "v_cvt_pk_bf16_f32 v97, v66, v67\n\t" \
    "v_exp_f32 v70, v70\n\t" \
    "v_exp_f32 v71, v71\n\t" \
    "v_exp_f32 v72, v72\n\t" \
    "v_exp_f32 v73, v73\n\t" \
    "v_cvt_pk_bf16_f32 v98, v68, v69\n\t" \
    "v_cvt_pk_bf16_f32 v99, v70, v71\n\t" \
    "v_exp_f32 v74, v74\n\t" \
    "v_exp_f32 v75, v75\n\t" \
    "s_waitcnt lgkmcnt(1)\n\t" \
    "v_mfma_f32_32x32x16_bf16 %[o], v[120:123], v[96:99], %[o]\n\t" \
    "s_mov_b64 exec, %[vm]\n\t" \
    "ds_read_b128 v[112:115], %[va] offset:2048\n\t" \
    "ds_read_b128 v[116:119], %[va] offset:2560\n\t" \
    "s_mov_b64 exec, -1\n\t" \
    "v_exp_f32 v76, v76\n\t" \
    "v_exp_f32 v77, v77\n\t" \
    "v_cvt_pk_bf16_f32 v100, v72, v73\n\t" \
    "v_cvt_pk_bf16_f32 v101, v74, v75\n\t" \
    "v_exp_f32 v78, v78\n\t" \
    "v_exp_f32 v79, v79\n\t" \
    "s_nop 0\n\t" \
    "v_cvt_pk_bf16_f32 v102, v76, v77\n\t" \
    "v_cvt_pk_bf16_f32 v103, v78, v79\n\t" \
    "v_exp_f32 v80, v80\n\t" \
    "v_exp_f32 v81, v81\n\t" \
    "v_mfma_f32_32x32x16_bf16 %[o], v[124:127], v[100:103], %[o]\n\t" \
    "s_waitcnt lgkmcnt(2)\n\t" \
    "v_mfma_f32_32x32x16_bf16 v[64:79], v[104:107], %[q], 0\n\t" \
    "v_exp_f32 v82, v82\n\t" \
    "v_exp_f32 v83, v83\n\t" \
    "v_exp_f32 v84, v84\n\t" \
    "v_exp_f32 v85, v85\n\t" \
    "v_cvt_pk_bf16_f32 v96, v80, v81\n\t" \
    "v_cvt_pk_bf16_f32 v97, v82, v83\n\t" \
    "v_exp_f32 v86, v86\n\t" \
    "v_exp_f32 v87, v87\n\t" \
    "v_exp_f32 v88, v88\n\t" \
    "v_exp_f32 v89, v89\n\t" \
    "v_cvt_pk_bf16_f32 v98, v84, v85\n\t" \
    "v_cvt_pk_bf16_f32 v99, v86, v87\n\t" \
    "v_exp_f32 v90, v90\n\t" \
    "v_exp_f32 v91, v91\n\t" \
    "v_mfma_f32_32x32x16_bf16 %[o2], v[120:123], v[96:99], %[o2]\n\t" \
    "ds_read_b128 v[108:111], %[ka] offset:3072\n\t" \
    "v_exp_f32 v92, v92\n\t" \
    "v_exp_f32 v93, v93\n\t" \
    "v_cvt_pk_bf16_f32 v100, v88, v89\n\t" \
    "v_cvt_pk_bf16_f32 v101, v90, v91\n\t" \
    "v_exp_f32 v94, v94\n\t" \
    "v_exp_f32 v95, v95\n\t" \
    "s_nop 0\n\t" \
    "v_cvt_pk_bf16_f32 v102, v92, v93\n\t" \
    "v_cvt_pk_bf16_f32 v103, v94, v95\n\t" \
    "v_exp_f32 v64, v64\n\t" \
    "v_exp_f32 v65, v65\n\t" \
    "v_mfma_f32_32x32x16_bf16 %[o2], v[124:127], v[100:103], %[o2]\n\t" \
    "s_add_u32 %[pc], %[pc], 1\n\t" \
    "v_mov_b32 %[pt], %[pc]\n\t" \
    "ds_write_b32 %[pa], %[pt]\n\t" \
    "ds_read_b32 %[pu], %[pb]\n\t" \
    "v_mfma_f32_32x32x16_bf16 v[80:95], v[104:107], %[q2], 0\n\t" \
    "v_exp_f32 v66, v66\n\t" \
    "v_exp_f32 v67, v67\n\t" \
    "v_exp_f32 v68, v68\n\t" \
    "v_exp_f32 v69, v69\n\t" \
    "v_cvt_pk_bf16_f32 v96, v64, v65\n\t" \
    "v_cvt_pk_bf16_f32 v97, v66, v67\n\t" \
    "v_exp_f32 v70, v70\n\t" \
    "v_exp_f32 v71, v71\n\t" \
    "v_exp_f32 v72, v72\n\t" \
    "v_exp_f32 v73, v73\n\t" \
    "v_cvt_pk_bf16_f32 v98, v68, v69\n\t" \
    "v_cvt_pk_bf16_f32 v99, v70, v71\n\t" \
    "v_exp_f32 v74, v74\n\t" \
    "v_exp_f32 v75, v75\n\t" \
    "s_waitcnt lgkmcnt(3)\n\t" \
    "v_mfma_f32_32x32x16_bf16 %[o], v[112:115], v[96:99], %[o]\n\t" \
    "s_mov_b64 exec, %[vm]\n\t" \
    "ds_read_b128 v[120:123], %[va] offset:3072\n\t" \
    "ds_read_b128 v[124:127], %[va] offset:3584\n\t" \
    "s_mov_b64 exec, -1\n\t" \
    "v_exp_f32 v76, v76\n\t" \
    "v_exp_f32 v77, v77\n\t" \
    "v_cvt_pk_bf16_f32 v100, v72, v73\n\t" \
    "v_cvt_pk_bf16_f32 v101, v74, v75\n\t" \
    "v_exp_f32 v78, v78\n\t" \
    "v_exp_f32 v79, v79\n\t" \
    "s_nop 0\n\t" \
    "v_cvt_pk_bf16_f32 v102, v76, v77\n\t" \
    "v_cvt_pk_bf16_f32 v103, v78, v79\n\t" \
    "v_exp_f32 v80, v80\n\t" \
    "v_exp_f32 v81, v81\n\t" \
    "v_mfma_f32_32x32x16_bf16 %[o], v[116:119], v[100:103], %[o]\n\t" \
    "s_waitcnt lgkmcnt(2)\n\t" \
    "v_readfirstlane_b32 %[ps], %[pu]\n\t" \
    "s_nop 0\n\t" \
    "s_cmp_gt_u32 %[pc], %[ps]\n\t" \
    "s_cbranch_scc1 8f\n\t" \
    "s_setprio 1\n\t" \
    "s_branch 9f\n\t" \
    "8:\n\t" \
    "s_setprio 0\n\t" \
    "9:\n\t" \
    "v_mfma_f32_32x32x16_bf16 v[64:79], v[108:111], %[q], 0\n\t" \
    "v_exp_f32 v82, v82\n\t" \
    "v_exp_f32 v83, v83\n\t" \
    "v_exp_f32 v84, v84\n\t" \
    "v_exp_f32 v85, v85\n\t" \
    "v_cvt_pk_bf16_f32 v96, v80, v81\n\t" \
    "v_cvt_pk_bf16_f32 v97, v82, v83\n\t" \
    "v_exp_f32 v86, v86\n\t" \
    "v_exp_f32 v87, v87\n\t" \
    "v_exp_f32 v88, v88\n\t" \
    "v_exp_f32 v89, v89\n\t" \
    "v_cvt_pk_bf16_f32 v98, v84, v85\n\t" \
    "v_cvt_pk_bf16_f32 v99, v86, v87\n\t" \
    "v_exp_f32 v90, v90\n\t" \
    "v_exp_f32 v91, v91\n\t" \
    "v_mfma_f32_32x32x16_bf16 %[o2], v[112:115], v[96:99], %[o2]\n\t" \
    "ds_read_b128 v[104:107], %[ka] offset:4096\n\t" \
    "v_exp_f32 v92, v92\n\t" \
    "v_exp_f32 v93, v93\n\t" \
    "v_cvt_pk_bf16_f32 v100, v88, v89\n\t" \
    "v_cvt_pk_bf16_f32 v101, v90, v91\n\t" \
    "v_exp_f32 v94, v94\n\t" \
    "v_exp_f32 v95, v95\n\t" \
    "s_nop 0\n\t" \
    "v_cvt_pk_bf16_f32 v102, v92, v93\n\t" \
    "v_cvt_pk_bf16_f32 v103, v94, v95\n\t" \
    "v_exp_f32 v64, v64\n\t" \
    "v_exp_f32 v65, v65\n\t" \
    "v_mfma_f32_32x32x16_bf16 %[o2], v[116:119], v[100:103], %[o2]\n\t" \
    "v_mfma_f32_32x32x16_bf16 v[80:95], v[108:111], %[q2], 0\n\t" \
    "v_exp_f32 v66, v66\n\t" \
    "v_exp_f32 v67, v67\n\t" \
    "v_exp_f32 v68, v68\n\t" \
    "v_exp_f32 v69, v69\n\t" \
    "v_cvt_pk_bf16_f32 v96, v64, v65\n\t" \
    "v_cvt_pk_bf16_f32 v97, v66, v67\n\t" \
    "v_exp_f32 v70, v70\n\t" \
    "v_exp_f32 v71, v71\n\t" \
    "v_exp_f32 v72, v72\n\t" \
    "v_exp_f32 v73, v73\n\t" \
    "v_cvt_pk_bf16_f32 v98, v68, v69\n\t" \
    "v_cvt_pk_bf16_f32 v99, v70, v71\n\t" \
    "v_exp_f32 v74, v74\n\t" \
    "v_exp_f32 v75, v75\n\t" \
    "s_waitcnt lgkmcnt(1)\n\t" \
    "v_mfma_f32_32x32x16_bf16 %[o], v[120:123], v[96:99], %[o]\n\t" \
    "s_mov_b64 exec, %[vm]\n\t" \
    "ds_read_b128 v[112:115], %[va] offset:4096\n\t" \
    "ds_read_b128 v[116:119], %[va] offset:4608\n\t" \
    "s_mov_b64 exec, -1\n\t" \
    "v_exp_f32 v76, v76\n\t" \
    "v_exp_f32 v77, v77\n\t" \
    "v_cvt_pk_bf16_f32 v100, v72, v73\n\t" \
    "v_cvt_pk_bf16_f32 v101, v74, v75\n\t" \
    "v_exp_f32 v78, v78\n\t" \
    "v_exp_f32 v79, v79\n\t" \
    "s_nop 0\n\t" \
    "v_cvt_pk_bf16_f32 v102, v76, v77\n\t" \
    "v_cvt_pk_bf16_f32 v103, v78, v79\n\t" \
    "v_exp_f32 v80, v80\n\t" \
    "v_exp_f32 v81, v81\n\t" \
    "v_mfma_f32_32x32x16_bf16 %[o], v[124:127], v[100:103], %[o]\n\t" \
    "s_waitcnt lgkmcnt(2)\n\t" \
    "v_mfma_f32_32x32x16_bf16 v[64:79], v[104:107], %[q], 0\n\t" \
    "v_exp_f32 v82, v82\n\t" \
    "v_exp_f32 v83, v83\n\t" \
    "v_exp_f32 v84, v84\n\t" \
    "v_exp_f32 v85, v85\n\t" \
    "v_cvt_pk_bf16_f32 v96, v80, v81\n\t" \
    "v_cvt_pk_bf16_f32 v97, v82, v83\n\t" \
    "v_exp_f32 v86, v86\n\t" \
    "v_exp_f32 v87, v87\n\t" \
    "v_exp_f32 v88, v88\n\t" \
    "v_exp_f32 v89, v89\n\t" \
    "v_cvt_pk_bf16_f32 v98, v84, v85\n\t" \
    "v_cvt_pk_bf16_f32 v99, v86, v87\n\t" \
    "v_exp_f32 v90, v90\n\t" \
    "v_exp_f32 v91, v91\n\t" \
    "v_mfma_f32_32x32x16_bf16 %[o2], v[120:123], v[96:99], %[o2]\n\t" \
    "ds_read_b128 v[108:111], %[ka] offset:5120\n\t" \
    "v_exp_f32 v92, v92\n\t" \
    "v_exp_f32 v93, v93\n\t" \
    "v_cvt_pk_bf16_f32 v100, v88, v89\n\t" \
    "v_cvt_pk_bf16_f32 v101, v90, v91\n\t" \
    "v_exp_f32 v94, v94\n\t" \
    "v_exp_f32 v95, v95\n\t" \
    "s_nop 0\n\t" \
    "v_cvt_pk_bf16_f32 v102, v92, v93\n\t" \
    "v_cvt_pk_bf16_f32 v103, v94, v95\n\t" \
    "v_exp_f32 v64, v64\n\t" \
    "v_exp_f32 v65, v65\n\t" \
    "v_mfma_f32_32x32x16_bf16 %[o2], v[124:127], v[100:103], %[o2]\n\t" \
    "v_add_u32 %[ka], 0x1000, %[ka]\n\t" \
    "v_add_u32 %[va], 0x1000, %[va]\n\t" \
    "s_add_u32 %[it], %[it], 1\n\t" \
    "s_cmp_lt_u32 %[it], 8\n\t" \
    "s_cbranch_scc1 1b\n\t" \
    "s_setprio 0\n\t" \
    "s_waitcnt lgkmcnt(0)\n\t" \
    "s_nop 11\n\t"
#define ATTN_CLOB "v64", "v65", "v66", "v67", "v68", "v69", "v70", "v71", "v72", "v73", "v74", "v75", "v76", "v77", "v78", "v79", "v80", "v81", "v82", "v83", "v84", "v85", "v86", "v87", "v88", "v89", "v90", "v91", "v92", "v93", "v94", "v95", "v96", "v97", "v98", "v99", "v100", "v101", "v102", "v103", "v104", "v105", "v106", "v107", "v108", "v109", "v110", "v111", "v112", "v113", "v114", "v115", "v116", "v117", "v118", "v119", "v120", "v121", "v122", "v123", "v124", "v125", "v126", "v127"

constexpr int AL_K = 0, AL_V = 65536, AL_Q = 131072, AL_X = AL_Q + 8192;
constexpr int AL_PROG = AL_X + 8 * 2304 + 4096;
constexpr int LDS_BYTES = 163840;

struct Ctx { char* ws; rsrc_t rs; unsigned lds0, voff; int lane, w, g, j; };
DI Ctx fresh(const Ctx& c0) { Ctx c = c0; asm volatile("" : "+s"(c.ws), "+s"(c.w), "+s"(c.g), "+s"(c.j));
  asm volatile("v_mbcnt_lo_u32_b32 %0, -1, 0\n\tv_mbcnt_hi_u32_b32 %0, -1, %0" : "=v"(c.lane)); c.voff = c.lane * 16; return c; }

template <bool S = true> DI void group_sync(const Ctx& c, int hid) {
  if constexpr (S) bar_lgkm();
  drain_bar();
  GAS unsigned* fl = (GAS unsigned*)(c.ws + OFF_SYNC + (hid * 32 + c.g) * 256);
  if (c.w == 1) { if (c.lane == 0) __hip_atomic_store(fl + c.j, 1u, RLX_AGENT); }
  if (c.w == 0) { __builtin_amdgcn_s_sleep(4); for (unsigned s = 0; s < SPIN_CAP; ++s) { if (__all(__hip_atomic_load(fl + (c.lane & 7), RLX_AGENT) != 0u)) break; __builtin_amdgcn_s_sleep(1); } }
  bar_lgkm();
}
DI void ld_a22(const Ctx& c, const char* WF, int nt0, int ch0) {
  const int lb = c.w & 1;
#pragma unroll
  for (int ch = ch0; ch < ch0 + 2; ++ch)
#pragma unroll
    for (int f = 0; f < 2; ++f) glds4<true>(WF + ((size_t)(nt0 + f) * KSD + lb * 16 + ch * 4) * 1024, c.voff, c.lds0 + (ch * 4 + lb * 2 + f) * 4096);
}
DI void wait_a22() { wait_vm_bar<24>(); wait_vm_bar<16>(); wait_vm_bar<8>(); wait_vm_bar<0>(); }
template <bool SC1> DI void ld_b22(const Ctx& c, const char* XF, int boff) {
  const int lb = c.w & 1;
  auto issue = [&](int ch) {
#pragma unroll
    for (int f = 0; f < 2; ++f) glds4<SC1>(XF + ((size_t)f * KSD + lb * 16 + ch * 4) * 1024, c.voff, c.lds0 + boff + (ch * 4 + lb * 2 + f) * 4096); };
  issue(0); issue(1); issue(2);
  wait_vm_bar<16>(); issue(3);
  wait_vm_bar<16>(); wait_vm_bar<8>(); wait_vm_bar<0>();
}
DI void burst_gap() { __builtin_amdgcn_s_sleep(2); }
template <class Pre>
DI void mm22(const Ctx& c, char* lds, int boff, f32x16& acc, Pre&& pre) {
  const int tl = c.w & 1, ng = (c.w >> 1) & 1, kh = c.w >> 2; const int rot = (c.lane ^ (kh << 4)) * 16;
  const LAS char* lA = (const LAS char*)lds + rot + (kh * 2 + ng) * 4096;
  const LAS char* lB = (const LAS char*)lds + boff + c.lane * 16 + (kh * 2 + tl) * 4096;
#pragma unroll
  for (int i = 0; i < 16; ++i) acc[i] = 0.f;
  static_for<0, 4>([&](auto ci) { constexpr int ch = decltype(ci)::value;
    bar_lgkm();
    bf16x8 bf[4], af[4];
#pragma unroll
    for (int i = 0; i < 4; ++i) { bf[i] = *(const LAS bf16x8*)(lB + ch * 16384 + i * 1024); af[i] = *(const LAS bf16x8*)(lA + ch * 16384 + i * 1024); }
    if constexpr (ch == 3) pre();
    __builtin_amdgcn_sched_barrier(0);
#pragma unroll
    for (int i = 0; i < 4; ++i) acc = MFMA(af[i], bf[i], acc);
  });
}
template <int ROLE>
DI void mm_kv(const Ctx& c, char* lds, f32x16& acc) {
  const int tl = c.w & 1, kh = c.w >> 2; const int rot = (c.lane ^ (kh << 4)) * 16;
  const LAS char* lK = (const LAS char*)lds + 65536 + (ROLE == 1 ? rot : c.lane * 16) + kh * 16384;
  const LAS char* lB = (const LAS char*)lds + 98304 + (ROLE == 2 ? rot : c.lane * 16) + (kh * 2 + tl) * 4096;
#pragma unroll
  for (int i = 0; i < 16; ++i) acc[i] = 0.f;
  static_for<0, 4>([&](auto ci) { constexpr int ch = decltype(ci)::value;
    bar_lgkm();
    if constexpr (ROLE != 0) {
      bf16x8 bf[4], kf[4];
#pragma unroll
      for (int i = 0; i < 4; ++i) { bf[i] = *(const LAS bf16x8*)(lB + ch * 16384 + i * 1024); kf[i] = *(const LAS bf16x8*)(lK + (ch * 4 + i) * 1024); }
      __builtin_amdgcn_sched_barrier(0);
#pragma unroll
      for (int i = 0; i < 4; ++i) acc = ROLE == 1 ? MFMA(kf[i], bf[i], acc) : MFMA(bf[i], kf[i], acc);
    }
  });
}
DI void mm_q16(const Ctx& c, char* lds, f32x16& acc) {
  const int tl = c.w & 1, ng = (c.w >> 1) & 1, kh = c.w >> 2; const int rot = (c.lane ^ (kh << 4)) * 16;
  const LAS char* lA = (const LAS char*)lds + rot + (kh * 2 + ng) * 4096;
  const LAS char* lB = (const LAS char*)lds + 98304 + c.lane * 16 + (kh * 2 + tl) * 4096;
#pragma unroll
  for (int i = 0; i < 16; ++i) acc[i] = 0.f;
#pragma unroll
  for (int ch = 0; ch < 4; ++ch) {
    bf16x8 bf[4], af[4];
#pragma unroll
    for (int i = 0; i < 4; ++i) { bf[i] = *(const LAS bf16x8*)(lB + ch * 16384 + i * 1024); af[i] = *(const LAS bf16x8*)(lA + ch * 16384 + i * 1024); }
#pragma unroll
    for (int i = 0; i < 4; ++i) acc = MFMA(af[i], bf[i], acc);
  }
}
DI void xsend(LAS char* xw, const f32x16& a) { *(LAS f32x4*)(xw) = f32x4{a[8], a[9], a[10], a[11]}; *(LAS f32x4*)(xw + 1024) = f32x4{a[12], a[13], a[14], a[15]}; }
DI void xfin(const LAS char* xr, const f32x16& acc, float* v) {
  const f32x4 a = *(const LAS f32x4*)(xr), b = *(const LAS f32x4*)(xr + 1024);
#pragma unroll
  for (int i = 0; i < 4; ++i) { v[i] = acc[i] + a[i]; v[4 + i] = acc[4 + i] + b[i]; }
}
DI void stat_store(const Ctx& c, int which, int frag, int tb, const u32x4& f) {
  float s = 0.f, q = 0.f;
#pragma unroll
  for (int i = 0; i < 4; ++i) { s = dot2(f[i], 0x3f803f80u, s); q = dot2(f[i], f[i], q); }
  s = add32(s); q = add32(q);
  __builtin_amdgcn_raw_buffer_store_b64(u32x2{__float_as_uint(s), __float_as_uint(q)}, c.rs, c.lane < 32 ? OFF_SP + which * 524288 + (frag * S_ + tb * 32 + c.lane) * 8 : 0x20000000u, 0, 16);
}
DI void stat_load(const Ctx& c, int which, int tb, u32x2* p) {
  const int qd = c.w >> 1, r = c.lane & 31, h = c.lane >> 5;
#pragma unroll
  for (int i = 0; i < 4; ++i) p[i] = __builtin_amdgcn_raw_buffer_load_b64(c.rs, OFF_SP + which * 524288 + ((8 * qd + 4 * h + i) * S_ + tb * 32 + r) * 8, 0, 16);
}
DI f32x2 stat_quarter(const u32x2* p) {
  float s = 0.f, q = 0.f;
#pragma unroll
  for (int i = 0; i < 4; ++i) { s += __uint_as_float(p[i][0]); q += __uint_as_float(p[i][1]); }
  return f32x2{add32(s), add32(q)};
}
DI u32x4 pack8(const float* v) { return u32x4{cvtpk(v[0], v[1]), cvtpk(v[2], v[3]), cvtpk(v[4], v[5]), cvtpk(v[6], v[7])}; }
DI void ln_fin(float s, float q, float& mean, float& rstd) { mean = s * (1.f / 512.f); rstd = __builtin_amdgcn_rsqf(fmaxf(q * (1.f / 512.f) - mean * mean, 0.f) + 1e-5f); }

constexpr int CODE_BYTES = 29696, CODE0_BYTES = 15360;
constexpr int Q0_XT = 0, Q0_WT = 65536, Q0_KT = 131072;
DI bf16x8 tr_frag(const LAS char* a, int second) {
  const s16x4_t lo = __builtin_amdgcn_ds_read_tr16_b64_v4i16((LAS s16x4_t*)a), hi = __builtin_amdgcn_ds_read_tr16_b64_v4i16((LAS s16x4_t*)(a + second));
  return __builtin_shufflevector(lo, hi, 0, 1, 2, 3, 4, 5, 6, 7);
}
template <int ROLE, int YOUNG, bool CS>
DI void mm_raw(const Ctx& c, char* lds, f32x16& acc, f32x16& csa, f32x16& kva, f32x16& kcs) {
  const int tl = c.w & 1, ng = (c.w >> 1) & 1, kh = c.w >> 2, lane = c.lane, r = lane & 31, h = lane >> 5;
  const int row = 32 * tl + r, m = (row >> 1) & 7;
  const LAS char* xb = (const LAS char*)lds + Q0_XT + (kh * 256 + row) * 128;
  unsigned xo[4];
#pragma unroll
  for (int e = 0; e < 4; ++e) xo[e] = ((2 * e + h) ^ m) * 16;
  const int xrot = ((r ^ (16 * kh)) - r) * 128;
  const int g16 = lane >> 4, q = (lane & 15) >> 2, p = lane & 3;
  const LAS char* wa = (const LAS char*)lds + Q0_WT + (256 * kh + 8 * h + q) * 128 + ((4 * ng + 2 * ((g16 & 1) ^ kh) + (p >> 1)) ^ (((q >> 1) & 1) << 2)) * 16 + (p & 1) * 8;
  const LAS char* ka = (const LAS char*)lds + Q0_KT + (256 * kh + 8 * h + q) * 64 + (ROLE == 1 ? ((g16 & 1) ^ kh) : (g16 & 1)) * 32 + p * 8;
  const bf16x8 ones = {0x3f80, 0x3f80, 0x3f80, 0x3f80, 0x3f80, 0x3f80, 0x3f80, 0x3f80};
#pragma unroll
  for (int i = 0; i < 16; ++i) { acc[i] = 0.f; csa[i] = 0.f; kva[i] = 0.f; kcs[i] = 0.f; }
  static_for<0, 4>([&](auto ci) { constexpr int ch = decltype(ci)::value;
    wait_vm_bar<2 * (3 - ch) + YOUNG>();
    bf16x8 bf[4], af[4], kf[4], br[4];
#pragma unroll
    for (int i = 0; i < 4; ++i) { const int ks = ch * 4 + i;
      bf[i] = *(const LAS bf16x8*)(xb + xo[i] + ch * 8192);
      af[i] = tr_frag(wa + ks * 2048, 512);
      if constexpr (ROLE != 0) kf[i] = tr_frag(ka + ks * 1024, 256);
      if constexpr (ROLE == 2) br[i] = *(const LAS bf16x8*)(xb + xrot + xo[i] + ch * 8192); }
    __builtin_amdgcn_sched_barrier(0);
#pragma unroll
    for (int i = 0; i < 4; ++i) {
      acc = MFMA(af[i], bf[i], acc);
      if constexpr (CS) csa = MFMA(af[i], ones, csa);
      if constexpr (ROLE == 1) { kva = MFMA(kf[i], bf[i], kva); kcs = MFMA(kf[i], ones, kcs); }
      if constexpr (ROLE == 2) { kva = MFMA(br[i], kf[i], kva); kcs = MFMA(ones, kf[i], kcs); }
    }
  });
}

extern "C" __global__ __launch_bounds__(768, 3) void qkv0_kernel(const unsigned short* x, const unsigned short* wq, const unsigned short* wk, const unsigned short* wv,
                                                      const unsigned short* w1, const unsigned short* b1, char* ws) {
  extern __shared__ __attribute__((aligned(16))) char lds[];
  Ctx c;
  c.ws = ws; c.rs = __builtin_amdgcn_make_buffer_rsrc(ws, 0, WS_USED, 0x00020000);
  c.lane = threadIdx.x & 63; c.w = __builtin_amdgcn_readfirstlane(threadIdx.x >> 6);
  c.lds0 = (unsigned)(uintptr_t)lds; c.voff = c.lane * 16;
  { const int b = blockIdx.x, xx = b & 7, y = b >> 3; c.g = 4 * xx + (y >> 3); c.j = y & 7; }
  const int lane = c.lane, w = c.w, g = c.g, j = c.j;
  LAS char* L = (LAS char*)lds;
  constexpr int ST = Q0_WT + 20480;
  if (w >= 8) {
    const int lw = w - 8, lb = lw & 1;
    float ss = 0.f, sq = 0.f, s2 = 0.f, q2 = 0.f;
    auto stats = [&](int ch) {
#pragma unroll
      for (int k = 0; k < 4; ++k) { const u32x4 u = *(const LAS u32x4*)(L + Q0_XT + ((lb * 4 + ch) * 64 + lane) * 128 + ((4 * (lw >> 1) + k) ^ ((lane >> 1) & 7)) * 16);
#pragma unroll
        for (int e = 0; e < 4; ++e) { const float a = bflo(u[e]), b = bfhi(u[e]); ss += a; s2 += b; sq = __builtin_fmaf(a, a, sq); q2 = __builtin_fmaf(b, b, q2); } } };
    __builtin_amdgcn_s_setprio(3);
    u32x4 codepf0 = {0, 0, 0, 0};
    { unsigned plo, phi;
      asm volatile("s_getpc_b64 s[40:41]\n\ts_add_u32 s40, s40, qkv0_kernel@rel32@lo+4\n\ts_addc_u32 s41, s41, qkv0_kernel@rel32@hi+12\n\ts_mov_b32 %0, s40\n\ts_mov_b32 %1, s41"
                   : "=s"(plo), "=s"(phi) :: "s40", "s41", "scc");
      const unsigned long long pc = ((unsigned long long)phi << 32) | plo;
      const unsigned o = imin((int)(((blockIdx.x >> 3) & 3) * 4 + lw) * 1024 + lane * 16, CODE0_BYTES - 16);
      asm volatile("global_load_dwordx4 %0, %1, %2" : "+v"(codepf0) : "v"(o), "s"(pc) : "memory"); }
    {
      const int sub = lw >> 1;
      const unsigned voffW = (lane >> 3) * 1024 + ((lane & 7) ^ (((lane >> 4) & 1) << 2)) * 16;
      const unsigned short* kvsrc = ((lane & 3) < 2 ? wv : wk) + (lane >> 2) * 16 + (lane & 1) * 8;
      auto issue = [&](int ch) { const int k0 = 256 * lb + 64 * ch;
#pragma unroll
        for (int p = 0; p < 4; ++p) glds1(wq + (size_t)(k0 + 8 * (4 * sub + p)) * 512 + 64 * j, voffW, c.lds0 + Q0_WT + (k0 + 8 * (4 * sub + p)) * 128);
        if (j < 2) {
#pragma unroll
          for (int p = 0; p < 2; ++p) glds1v(kvsrc + (k0 + 16 * (2 * sub + p)) * 16, c.lds0 + Q0_KT + (k0 + 16 * (2 * sub + p)) * 64); } };
      issue(0); issue(1); issue(2); issue(3);
      u32x4 codepf = {0, 0, 0, 0};
      { unsigned plo, phi;
        asm volatile("s_getpc_b64 s[40:41]\n\ts_add_u32 s40, s40, mega_kernel@rel32@lo+4\n\ts_addc_u32 s41, s41, mega_kernel@rel32@hi+12\n\ts_mov_b32 %0, s40\n\ts_mov_b32 %1, s41"
                     : "=s"(plo), "=s"(phi) :: "s40", "s41", "scc");
        const unsigned long long pc = ((unsigned long long)phi << 32) | plo;
        const unsigned o = imin((int)(((blockIdx.x >> 3) & 7) * 4 + lw) * 1024 + lane * 16, CODE_BYTES - 16);
        asm volatile("global_load_dwordx4 %0, %1, %2" : "+v"(codepf) : "v"(o), "s"(pc) : "memory"); }
      if (j < 2) wait_vm_bar<19>(); else wait_vm_bar<13>();
      stats(0);
      if (j < 2) wait_vm_bar<13>(); else wait_vm_bar<9>();
      stats(1);
      if (j < 2) wait_vm_bar<7>(); else wait_vm_bar<5>();
      stats(2);
      wait_vm_bar<1>();
      stats(3);
      asm volatile("s_waitcnt vmcnt(0)" : "+v"(codepf), "+v"(codepf0) :: "memory");
    }
    *(LAS f32x2*)(L + ST + (lw * 64 + lane) * 8) = f32x2{ss + s2, sq + q2};
    bar_lgkm();
    return;
  }
  { const unsigned i = blockIdx.x * 512 + threadIdx.x; if (i < SYNC_BYTES / 16) *(GAS u32x4*)(ws + OFF_SYNC + i * 16) = u32x4{0, 0, 0, 0}; }
  { const unsigned voffX = (lane >> 3) * 1024 + ((lane & 7) ^ (((w & 1) * 4 + (lane >> 4)) & 7)) * 16;
#pragma unroll
    for (int ch = 0; ch < 4; ++ch)
#pragma unroll
      for (int k2 = 0; k2 < 2; ++k2) glds1(x + (size_t)(64 * g + 8 * w) * 512 + 256 * k2 + 64 * ch, voffX, c.lds0 + Q0_XT + ((k2 * 4 + ch) * 64 + 8 * w) * 128); }
  const int side = j >= 2 ? g * 6 + (j - 2) : 1000;
  auto project = [&](auto sidec) { constexpr bool SIDE = decltype(sidec)::value;
  u32x4 tv[8];
  if constexpr (SIDE) {
#pragma unroll
    for (int k = 0; k < 8; ++k) tv[k] = gld<u32x4>(w1 + (size_t)(w * 64 + (lane >> 3) * 8 + k) * F_ + imin(side * 64 + (lane & 7) * 8, F_ - 8));
  }
  const int tl = w & 1, ng = (w >> 1) & 1, kh = w >> 2, r = lane & 31, h = lane >> 5, tb = 2 * g + tl, fo = 16 * kh + 4 * h;
  const bool kvme = !SIDE && (j < 2) && (tl == j);
  f32x16 acc, csa, kva, kcs;
  const int tlc = j < 2 ? (j ^ 1) : 1;
  if (tl == tlc) mm_raw<0, SIDE ? 8 : 0, true>(c, lds, acc, csa, kva, kcs);
  else if (SIDE || !kvme) mm_raw<0, SIDE ? 8 : 0, false>(c, lds, acc, csa, kva, kcs);
  else if (ng == 0) mm_raw<1, 0, false>(c, lds, acc, csa, kva, kcs); else mm_raw<2, 0, false>(c, lds, acc, csa, kva, kcs);
  auto xslot = [&](int ww) { return L + Q0_WT + (ww >> 2) * 32768 + (ww & 3) * 4352; };
  LAS char* xw = xslot(w); const LAS char* xr = xslot(w ^ 4);
  xsend(xw + lane * 16, acc);
  if (tl == tlc && r == 0) {
#pragma unroll
    for (int i = 0; i < 4; ++i) *(LAS f32x4*)(xw + 4224 + h * 64 + i * 16) = f32x4{csa[4 * i], csa[4 * i + 1], csa[4 * i + 2], csa[4 * i + 3]}; }
  if (kvme) {
    xsend(xw + 2048 + lane * 16, kva);
    if (ng == 0) { if (r == 0) { *(LAS f32x4*)(xw + 4160 + h * 32) = f32x4{kcs[8], kcs[9], kcs[10], kcs[11]}; *(LAS f32x4*)(xw + 4160 + h * 32 + 16) = f32x4{kcs[12], kcs[13], kcs[14], kcs[15]}; } }
    else if (h == 0) *(LAS float*)(xw + 4160 + r * 4) = kcs[0];
  }
  bar_lgkm();
  float mean, rstd;
  { f32x2 o = *(const LAS f32x2*)(L + ST + (32 * tl + r) * 8);
#pragma unroll
    for (int i = 1; i < 4; ++i) o += *(const LAS f32x2*)(L + ST + (i * 64 + 32 * tl + r) * 8);
    ln_fin(o[0], o[1], mean, rstd); }
  float v[8], cs[8];
  xfin(xr + lane * 16, acc, v);
  { const LAS char* own = xslot(tlc + 2 * ng + 4 * kh) + 4224 + h * 64; const LAS char* oth = xslot(tlc + 2 * ng + 4 * (kh ^ 1)) + 4224 + h * 64 + 32;
    const f32x4 a0 = *(const LAS f32x4*)(own), a1 = *(const LAS f32x4*)(own + 16), b0 = *(const LAS f32x4*)(oth), b1_ = *(const LAS f32x4*)(oth + 16);
#pragma unroll
    for (int i = 0; i < 4; ++i) { cs[i] = a0[i] + b0[i]; cs[4 + i] = a1[i] + b1_[i]; } }
  if (g == 0 && tl == 0 && r == 0) { float* d = (float*)(ws + OFF_CSQ) + (2 * j + ng) * 32 + fo; *(GAS f32x4*)d = f32x4{cs[0], cs[1], cs[2], cs[3]}; *(GAS f32x4*)(d + 8) = f32x4{cs[4], cs[5], cs[6], cs[7]}; }
  { const float a = rstd * (0.25f * 1.4426950408889634f), bb = -mean * a;
#pragma unroll
    for (int i = 0; i < 8; ++i) v[i] = v[i] * a + bb * cs[i]; }
  *(GAS u32x4*)(ws + OFF_QF + ((size_t)tb * KSD + 4 * j + 2 * ng + kh) * 1024 + lane * 16) = pack8(v);
  if (kvme) {
    xfin(xr + 2048 + lane * 16, kva, v);
    if (ng == 0) { if (kh == 1) {
        const f32x4 a = *(const LAS f32x4*)(xr + 4160 + h * 32), b = *(const LAS f32x4*)(xr + 4160 + h * 32 + 16);
#pragma unroll
        for (int i = 0; i < 4; ++i) { cs[i] = kcs[i] + a[i]; cs[4 + i] = kcs[4 + i] + b[i]; }
        if (g == 0 && j == 0 && r == 0) { float* d = (float*)(ws + OFF_CSQ) + 528 + 4 * h; *(GAS f32x4*)d = f32x4{cs[0], cs[1], cs[2], cs[3]}; *(GAS f32x4*)(d + 8) = f32x4{cs[4], cs[5], cs[6], cs[7]}; }
        const float b3 = -mean * rstd;
#pragma unroll
        for (int i = 0; i < 8; ++i) v[i] = v[i] * rstd + b3 * cs[i];
        *(GAS u32x4*)(ws + OFF_KF + tb * 1024 + lane * 16) = pack8(v); }
    } else {
      const float csd = kcs[0] + *(const LAS float*)(xr + 4160 + r * 4);
      if (g == 0 && j == 0 && kh == 0 && lane < 16) *(GAS float*)((float*)(ws + OFF_CSQ) + 512 + lane) = csd;
#pragma unroll
      for (int i = 0; i < 8; ++i) { const int t = 16 * kh + 8 * (i >> 2) + 4 * h + (i & 3); const float m = __int_as_float(__builtin_amdgcn_ds_bpermute(t * 4, __float_as_int(mean))), rs = __int_as_float(__builtin_amdgcn_ds_bpermute(t * 4, __float_as_int(rstd))); v[i] = (v[i] - m * csd) * rs; }
      if (r < 16) *(GAS u32x4*)(ws + OFF_VF + tb * 1024 + kh * 512 + (h * 16 + r) * 16) = pack8(v);
    }
  }
  if constexpr (SIDE) {
    float sm[8];
#pragma unroll
    for (int i = 0; i < 8; ++i) sm[i] = 0.f;
#pragma unroll
    for (int k = 0; k < 8; ++k)
#pragma unroll
      for (int i = 0; i < 4; ++i) { sm[2 * i] += bflo(tv[k][i]); sm[2 * i + 1] += bfhi(tv[k][i]); }
#pragma unroll
    for (int i = 0; i < 8; ++i)
#pragma unroll
      for (int mk = 8; mk < 64; mk <<= 1) sm[i] += __int_as_float(__builtin_amdgcn_ds_bpermute((lane ^ mk) * 4, __float_as_int(sm[i])));
    if (lane < 8) { *(LAS f32x4*)(L + Q0_KT + (w * 64 + lane * 8) * 4) = f32x4{sm[0], sm[1], sm[2], sm[3]}; *(LAS f32x4*)(L + Q0_KT + (w * 64 + lane * 8) * 4 + 16) = f32x4{sm[4], sm[5], sm[6], sm[7]}; }
    bar_lgkm();
    if (w == 0) { const int n = side * 64 + lane; float t = 0.f;
#pragma unroll
      for (int k = 0; k < 8; ++k) t += *(const LAS float*)(L + Q0_KT + (k * 64 + lane) * 4);
      if (n < F_) *(GAS f32x2*)(ws + OFF_CS1 + n * 8) = f32x2{t, bf2f(*(const GAS unsigned short*)(b1 + n))}; }
  }
  };
  if (side < 22) project(std::true_type{}); else project(std::false_type{});
}

extern "C" __global__ __launch_bounds__(768, 3) void mega_kernel(char* ws, const unsigned short* x, const unsigned short* wq, const unsigned short* wk, const unsigned short* wv, const unsigned short* wo, const unsigned short* w1, const unsigned short* w2, const unsigned short* b2, unsigned short* out) {
  extern __shared__ __attribute__((aligned(16))) char lds[];
  Ctx c0;
  c0.ws = ws; c0.rs = __builtin_amdgcn_make_buffer_rsrc(ws, 0, WS_USED, 0x00020000);
  c0.lane = 0; c0.w = __builtin_amdgcn_readfirstlane(threadIdx.x >> 6);
  c0.lds0 = (unsigned)(uintptr_t)lds; c0.voff = 0;
  { const int b = blockIdx.x, x = b & 7, y = b >> 3; c0.g = 4 * x + (y >> 3); c0.j = y & 7; }
  const int w0 = c0.w;

  if (w0 >= 8) {
    const bool isA = w0 >= 10;
    for (int layer = 0; layer < 2; ++layer) {
      { const Ctx c = fresh(c0); const int lw = c.w - 8, lb = lw & 1, kind = lw >> 1;
        const char* src = c.ws + (kind ? OFF_VF : OFF_KF) + layer * 65536 + lb * 32768; const unsigned dst = c.lds0 + (kind ? AL_V : AL_K) + lb * 32768;
        auto issue = [&](int ch) { glds4<true>(src + ch * 4096, c.voff, dst + ch * 4096); };
        if (layer == 0 && kind == 0) glds4<false>(c.ws + OFF_QF + ((size_t)(2 * c.g + lb) * KSD + 4 * c.j) * 1024, c.voff, c.lds0 + AL_Q + lb * 4096);
        issue(0); issue(1); issue(2); issue(3);
        wait_vm_bar<8>();
        issue(4); issue(5); issue(6); issue(7);
        wait_vm_bar<0>();
        if (layer == 0) {
          __builtin_amdgcn_s_setprio(3);
          const int u = blockIdx.x * 4 + lw;
          u32x4 v[4]; unsigned dst[4];
#pragma unroll
          for (int q = 0; q < 4; ++q) { const int f = imin(u + 1024 * q, 3807);
            const unsigned short* W; int ldw, ks, nt; unsigned lo = (c.lane & 3) * 8;
            if (f < 512) { W = wo; ldw = 512; nt = f >> 5; ks = f & 31; dst[q] = OFF_WO + f * 1024; }
            else if (f < 1888) { const int g2 = f - 512; W = w1; ldw = F_; nt = g2 >> 5; ks = g2 & 31; dst[q] = OFF_W1 + g2 * 1024; }
            else if (f < 3264) { const int g2 = f - 1888; W = w2; ldw = 512; nt = g2 / KSF; ks = g2 - nt * KSF; dst[q] = OFF_W2 + g2 * 1024; }
            else { const int g2 = f - 3264; W = wq; ldw = 512; nt = g2 >> 5; ks = g2 & 31; dst[q] = OFF_WQKV + g2 * 1024; }
            if (u + 1024 * q > 3807) dst[q] = 0x20000000u;
            if (f >= 3264 + 512) v[q] = gld<u32x4>(((c.lane & 3) < 2 ? wv : wk) + (16 * ks + (c.lane >> 2)) * 16 + (c.lane & 1) * 8);
            else v[q] = gld<u32x4>(W + (size_t)(16 * ks) * ldw + nt * 32 + (unsigned)((c.lane >> 2) * ldw + lo)); }
          LAS char* blk = (LAS char*)lds + AL_X + 8 * 2304 + lw * 1024;
#pragma unroll
          for (int q = 0; q < 4; ++q) { *(LAS u32x4*)(blk + c.voff) = v[q]; asm volatile("s_waitcnt lgkmcnt(0)" ::: "memory");
            const u32x4 fr = lds_block_to_frag(blk, c.lane); st_sc1(c.rs, dst[q] + c.voff, fr); asm volatile("s_waitcnt lgkmcnt(0)" ::: "memory"); }
          asm volatile("s_waitcnt vmcnt(0)" ::: "memory");
          GAS unsigned* rdy = (GAS unsigned*)(c.ws + OFF_WRDY);
          if (c.lane == 0) __hip_atomic_fetch_add(rdy + (blockIdx.x & 31) * 64, 1u, RLX_AGENT);
          if (lw == 0)
            for (unsigned s = 0; s < SPIN_CAP; ++s) { if (__all(__hip_atomic_load(rdy + (c.lane & 31) * 64, RLX_AGENT) >= 32u)) break; __builtin_amdgcn_s_sleep(16); }
          __builtin_amdgcn_s_setprio(0);
        }
        if (layer == 1 || lw == 0) {
          const int j = c.j, gq = c.g & 3, st = layer == 1 ? 16 : 4, f0 = layer == 1 ? gq + 4 * lw : gq; const int nt1 = j < 3 ? 6 * j : 18 + 5 * (j - 3);
          u32x4 dummy = {0, 0, 0, 0};
          __builtin_amdgcn_s_setprio(3);
          auto touch = [&](const char* base, int nfrag) {
            for (int f = f0; f < nfrag; f += st) asm volatile("global_load_dwordx4 %0, %1, %2 sc1" : "+v"(dummy) : "v"(c.voff), "s"(base + (size_t)f * 1024) : "memory");
            asm volatile("s_waitcnt vmcnt(0)" : "+v"(dummy) :: "memory"); };
          touch(c.ws + OFF_WO + (size_t)(2 * j) * KSD * 1024, 64);
          touch(c.ws + OFF_W1 + (size_t)nt1 * KSD * 1024, j < 3 ? 192 : 160);
          touch(c.ws + OFF_W2 + (size_t)(2 * j) * KSF * 1024, 172);
          __builtin_amdgcn_s_setprio(0); }
      }
      bar_lgkm();
      { const Ctx c = fresh(c0);
        bar_lgkm(); if (isA) ld_a22(c, c.ws + OFF_WO, 2 * c.j, 0); bar_lgkm(); if (isA) { burst_gap(); ld_a22(c, c.ws + OFF_WO, 2 * c.j, 2); } bar_lgkm();
        if (isA) wait_a22(); else ld_b22<true>(c, c.ws + OFF_OF + layer * 2 * MB + (size_t)(2 * c.g) * KSD * 1024, 65536); }
      bar_lgkm();
      { const Ctx c = fresh(c0); const int lb = c.w & 1, j = c.j;
        const int nt1 = j < 3 ? 6 * j : 18 + 5 * (j - 3), nt1max = j < 3 ? nt1 + 5 : nt1 + 4;
        if (isA) {
          auto issueA = [&](int s) { const int t = s >> 1, kc = s & 1;
#pragma unroll
            for (int q = 0; q < 4; ++q) { const int ng = q >> 1, i4 = (q & 1) * 4; const int nt = imin(nt1 + ng * 3 + t, nt1max);
              glds4<true>(c.ws + OFF_W1 + ((size_t)nt * KSD + lb * 16 + kc * 8 + i4) * 1024, c.voff, c.lds0 + (s % 3) * 32768 + ((ng * 2 + lb) * 8 + i4) * 1024); } };
          bar_lgkm(); issueA(0); bar_lgkm(); burst_gap(); issueA(1); bar_lgkm();
          wait_vm_bar<16>(); issueA(2);
          wait_vm_bar<16>(); issueA(3);
          wait_vm_bar<16>(); issueA(4);
          wait_vm_bar<16>(); issueA(5);
          const char* w2src = c.ws + OFF_W2 + ((size_t)(2 * c.j) * KSF + lb * 43) * 1024;
          auto issueW2 = [&](int ch) {
#pragma unroll
            for (int f = 0; f < 2; ++f) glds4<true>(w2src + ((size_t)f * KSF + ch * 4) * 1024, c.voff, c.lds0 + ch * 16384 + (lb * 2 + f) * 4096); };
          wait_vm_bar<16>(); issueW2(0); issueW2(1);
          wait_vm_bar<16>(); issueW2(2); issueW2(3);
        } else {
          bar_lgkm(); bar_lgkm(); bar_lgkm();
          const char* XF = c.ws + OFF_X1F + layer * 2 * MB + (size_t)(2 * c.g) * KSD * 1024;
#pragma unroll
          for (int kc = 0; kc < 2; ++kc)
#pragma unroll
            for (int q = 0; q < 4; ++q) { const int f = q >> 1, i4 = (q & 1) * 4;
              glds4<true>(XF + ((size_t)f * KSD + lb * 16 + kc * 8 + i4) * 1024, c.voff, c.lds0 + 98304 + kc * 32768 + ((lb * 2 + f) * 8 + i4) * 1024); }
          wait_vm_bar<16>(); wait_vm_bar<0>();
          if (lb == 0) glds4<false>(c.ws + OFF_CS1 + nt1 * 256, c.voff, c.lds0 + 98304 + 20480);
          wait_vm_bar<0>(); bar_lgkm(); bar_lgkm(); bar_lgkm();
        }
        bar_lgkm();
      }
      { const Ctx c = fresh(c0); const int lb = c.w & 1;
        if (isA) {
          const char* src = c.ws + OFF_W2 + ((size_t)(2 * c.j) * KSF + lb * 43) * 1024;
          auto issueA = [&](int ch) {
#pragma unroll
            for (int f = 0; f < 2; ++f) glds4<true>(src + ((size_t)f * KSF + ch * 4) * 1024, c.voff, c.lds0 + (ch % 4) * 16384 + (lb * 2 + f) * 4096); };
          bar_lgkm(); bar_lgkm();
          wait_vm_bar<24>();
          wait_vm_bar<16>(); issueA(4);
          wait_vm_bar<16>(); issueA(5);
          wait_vm_bar<16>(); issueA(6);
          wait_vm_bar<16>(); issueA(7);
          wait_vm_bar<16>(); issueA(8);
          wait_vm_bar<16>(); issueA(9);
          wait_vm_bar<16>(); issueA(10);
          wait_vm_bar<16>(); wait_vm_bar<8>(); wait_vm_bar<0>();
        } else {
          const char* src = c.ws + OFF_HF + layer * 6 * MB + ((size_t)(2 * c.g) * KSF + lb * 43) * 1024;
          auto issueB = [&](int ch) {
#pragma unroll
            for (int f = 0; f < 2; ++f) glds4<true>(src + ((size_t)f * KSF + ch * 4) * 1024, c.voff, c.lds0 + 65536 + (ch % 6) * 16384 + (lb * 2 + f) * 4096); };
          bar_lgkm(); bar_lgkm();
          issueB(0); issueB(1); issueB(2);
          wait_vm_bar<16>(); issueB(3); issueB(4);
          wait_vm_bar<24>(); issueB(5); issueB(6);
          wait_vm_bar<32>(); issueB(7);
          wait_vm_bar<32>(); issueB(8);
          wait_vm_bar<32>(); issueB(9);
          wait_vm_bar<32>(); issueB(10);
          wait_vm_bar<32>(); wait_vm_bar<24>(); wait_vm_bar<16>(); wait_vm_bar<8>(); wait_vm_bar<0>();
        }
        bar_lgkm();
      }
      if (layer == 0) {
        const Ctx c = fresh(c0); const int lb = c.w & 1;
        if (c.j < 2) {
          if (isA) {
            bar_lgkm();
#pragma unroll
            for (int q = 0; q < 4; ++q) glds4<true>(c.ws + OFF_WQKV + ((size_t)16 * KSD + lb * 16 + q * 4) * 1024, c.voff, c.lds0 + 65536 + (lb * 16 + q * 4) * 1024);
            bar_lgkm(); burst_gap(); ld_a22(c, c.ws + OFF_WQKV, 2 * c.j, 0); bar_lgkm();
            burst_gap(); ld_a22(c, c.ws + OFF_WQKV, 2 * c.j, 2);
            wait_vm_bar<32>(); bar_lgkm(); bar_lgkm(); bar_lgkm();
            bar_lgkm();
            wait_vm_bar<0>();
          } else {
            bar_lgkm(); bar_lgkm(); bar_lgkm();
            ld_b22<true>(c, c.ws + OFF_X2F + (size_t)(2 * c.g) * KSD * 1024, 98304);
            bar_lgkm(); bar_lgkm();
          }
          bar_lgkm();
        } else {
          bar_lgkm(); if (isA) ld_a22(c, c.ws + OFF_WQKV, 2 * c.j, 0); bar_lgkm(); if (isA) { burst_gap(); ld_a22(c, c.ws + OFF_WQKV, 2 * c.j, 2); } bar_lgkm();
          if (isA) wait_a22(); else ld_b22<true>(c, c.ws + OFF_X2F + (size_t)(2 * c.g) * KSD * 1024, 98304);
          bar_lgkm();
        }
        bar_lgkm();
      }
    }
    return;
  }

  LAS char* L = (LAS char*)lds;
  u32x4 resid;
  { const Ctx c = fresh(c0); const unsigned short* xr = x + (size_t)(64 * c.g + 32 * (w0 & 1) + (c.lane & 31)) * 512 + (2 * c.j + ((w0 >> 1) & 1)) * 32 + 16 * (w0 >> 2) + 4 * (c.lane >> 5);
    const u32x2 a = gld<u32x2>(xr), b = gld<u32x2>(xr + 8); resid = u32x4{a[0], a[1], b[0], b[1]}; }
  for (int layer = 0; layer < 2; ++layer) {
    {
      const Ctx c = fresh(c0); const int lane = c.lane, w = c.w, g = c.g, j = c.j, tl = w & 1, ng = (w >> 1) & 1, kh = w >> 2, r = lane & 31, h = lane >> 5, tb = 2 * g + tl, fo = 16 * kh + 4 * h; char* ws = c.ws; (void)r; (void)fo; (void)tb; (void)ws; (void)ng;
      const int hd = w & 3, kk = w >> 2;
      f32x16 o, o2;
      { float z = 0.f; asm volatile("" : "+v"(z));
#pragma unroll
        for (int i = 0; i < 16; ++i) { o[i] = z; o2[i] = z; } }
      {
        unsigned ka = c.lds0 + AL_K + kk * 32768 + lane * 16;
        unsigned va = c.lds0 + AL_V + kk * 32768 + ((lane >> 5) * 16 + (lane & 15)) * 16;
        const unsigned qa = c.lds0 + AL_Q + (kk * 4 + hd) * 1024 + lane * 16, qb = c.lds0 + AL_Q + ((kk ^ 1) * 4 + hd) * 1024 + lane * 16;
        const unsigned cst = ((lane & 27) == 16) ? 0x3f803f80u : 0u;
        const unsigned long long vm = 0x0000ffff0000ffffull;
        bf16x8 q, q2; int it, pc, ps; unsigned pt, pu;
        const unsigned pa = c.lds0 + AL_PROG + w * 4, pb = c.lds0 + AL_PROG + (w ^ 4) * 4;
        asm volatile(ATTN_ASM
                   : [o] "+&v"(o), [o2] "+&v"(o2), [ka] "+&v"(ka), [va] "+&v"(va), [q] "=&v"(q), [q2] "=&v"(q2), [it] "=&s"(it), [pc] "=&s"(pc), [ps] "=&s"(ps), [pt] "=&v"(pt), [pu] "=&v"(pu)
                   : [qa] "v"(qa), [qb] "v"(qb), [cst] "v"(cst), [vm] "s"(vm), [pa] "v"(pa), [pb] "v"(pb)
                   : "memory", "scc", ATTN_CLOB);
      }
      LAS char* xw = L + AL_X + w * 2304; const LAS char* xr = L + AL_X + (w ^ 4) * 2304;
      *(LAS f32x4*)(xw + lane * 16) = f32x4{o2[0], o2[1], o2[2], o2[3]}; *(LAS f32x4*)(xw + 1024 + lane * 16) = f32x4{o2[4], o2[5], o2[6], o2[7]}; *(LAS float*)(xw + 2048 + lane * 4) = o2[8];
      bar_lgkm();
      { const f32x4 a = *(const LAS f32x4*)(xr + lane * 16), cc = *(const LAS f32x4*)(xr + 1024 + lane * 16); const float l2 = *(const LAS float*)(xr + 2048 + lane * 4);
#pragma unroll
        for (int i = 0; i < 4; ++i) { o[i] += a[i]; o[4 + i] += cc[i]; }
        o[8] += l2; }
      float l = o[8];
      const float amag = ((__builtin_fabsf(o[0]) + __builtin_fabsf(o[1])) + (__builtin_fabsf(o[2]) + __builtin_fabsf(o[3]))) + ((__builtin_fabsf(o[4]) + __builtin_fabsf(o[5])) + (__builtin_fabsf(o[6]) + __builtin_fabsf(o[7])));
      const bool ok = (l >= 0x1p-60f) && (l <= 0x1p100f) && (amag <= 3.0e38f);
      if (!__all(ok)) {
        const LAS char* l3 = (const LAS char*)lds;
        const bf16x8 q = *(const LAS bf16x8*)(l3 + AL_Q + (kk * 4 + hd) * 1024 + lane * 16);
        const LAS char* kp = l3 + AL_K + lane * 16;
        f32x16 z;
#pragma unroll
        for (int i = 0; i < 16; ++i) z[i] = 0.f;
        float m = -INFINITY;
        for (int t = 0; t < 64; ++t) {
          f32x16 s = MFMA(*(const LAS bf16x8*)(kp + t * 1024), q, z);
#pragma unroll
          for (int i = 0; i < 16; ++i) m = fmaxf(m, s[i]);
        }
        m = max32(m);
#pragma unroll
        for (int i = 0; i < 16; ++i) { z[i] = -m; o[i] = 0.f; }
        const bool vl = (lane & 16) == 0; const unsigned cst = ((lane & 27) == 16) ? 0x3f803f80u : 0u;
        const LAS char* vp = l3 + AL_V + ((lane >> 5) * 16 + (lane & 15)) * 16;
        for (int t = 0; t < 64; ++t) {
          f32x16 s = MFMA(*(const LAS bf16x8*)(kp + t * 1024), q, z);
          u32x4 v0 = *(const LAS u32x4*)(vp + t * 1024), v1 = *(const LAS u32x4*)(vp + t * 1024 + 512);
          if (!vl) { v0 = u32x4{cst, cst, cst, cst}; v1 = v0; }
          u32x4 p0, p1;
#pragma unroll
          for (int i = 0; i < 4; ++i) { p0[i] = cvtpk(__builtin_amdgcn_exp2f(s[2 * i]), __builtin_amdgcn_exp2f(s[2 * i + 1])); p1[i] = cvtpk(__builtin_amdgcn_exp2f(s[8 + 2 * i]), __builtin_amdgcn_exp2f(s[9 + 2 * i])); }
          o = MFMA(__builtin_bit_cast(bf16x8, v0), __builtin_bit_cast(bf16x8, p0), o);
          o = MFMA(__builtin_bit_cast(bf16x8, v1), __builtin_bit_cast(bf16x8, p1), o);
        }
        l = o[8];
      }
      const float rl = __builtin_amdgcn_rcpf(l);
      st_sc1(c.rs, OFF_OF + layer * 2 * MB + ((2 * g + kk) * KSD + 4 * j + hd) * 1024 + lane * 16,
             u32x4{cvtpk(o[0] * rl, o[1] * rl), cvtpk(o[2] * rl, o[3] * rl), cvtpk(o[4] * rl, o[5] * rl), cvtpk(o[6] * rl, o[7] * rl)});
      group_sync(c, layer * 4 + 0);
    }
    {
      const Ctx c = fresh(c0); const int lane = c.lane, w = c.w, g = c.g, j = c.j, tl = w & 1, ng = (w >> 1) & 1, kh = w >> 2, r = lane & 31, h = lane >> 5, tb = 2 * g + tl, fo = 16 * kh + 4 * h; char* ws = c.ws; (void)r; (void)fo; (void)tb; (void)ws; (void)ng;
      f32x16 acc;
      mm22(c, lds, 65536, acc, []() {});
      LAS char* xw = L + AL_X + w * 2048; const LAS char* xr = L + AL_X + (w ^ 4) * 2048;
      xsend(xw + lane * 16, acc);
      bar_lgkm();
      float v[8]; xfin(xr + lane * 16, acc, v);
#pragma unroll
      for (int i = 0; i < 4; ++i) { v[2 * i] += bflo(resid[i]); v[2 * i + 1] += bfhi(resid[i]); }
      resid = pack8(v);
      st_sc1(c.rs, OFF_X1F + layer * 2 * MB + (tb * KSD + 2 * (2 * j + ng) + kh) * 1024 + lane * 16, resid);
      stat_store(c, 1 + 2 * layer, 2 * (2 * j + ng) + kh, tb, resid);
      group_sync(c, layer * 4 + 1);
    }
    {
      const Ctx c = fresh(c0); const int lane = c.lane, w = c.w, g = c.g, j = c.j, tl = w & 1, ng = (w >> 1) & 1, kh = w >> 2, r = lane & 31, h = lane >> 5, tb = 2 * g + tl, fo = 16 * kh + 4 * h; char* ws = c.ws; (void)r; (void)fo; (void)tb; (void)ws; (void)ng;
      const int nt1 = j < 3 ? 6 * j : 18 + 5 * (j - 3), nt1max = j < 3 ? nt1 + 5 : nt1 + 4;
      const float* cs = (const float*)(ws + OFF_CS1);
      const LAS char* lA = (const LAS char*)lds + (lane ^ (kh << 4)) * 16 + (ng * 2 + kh) * 8192;
      const LAS char* lA2 = lA + 65536; asm volatile("" : "+v"(lA2));
      const LAS char* lB = (const LAS char*)lds + 98304 + lane * 16 + (kh * 2 + tl) * 8192;
      bf16x8 bq[16]; f32x16 acc; float keep[8]; float mean = 0.f, rstd = 0.f; u32x2 sp[4];
      stat_load(c, 1 + 2 * layer, tb, sp);
      u32x4 hpack; const bool has3 = nt1 + ng * 3 + 2 <= nt1max;
      const LAS char* lT = (const LAS char*)lds + 98304 + 20480 + (ng * 96 + fo) * 8;
      auto hstore = [&](int t) { const int nt = nt1 + ng * 3 + t; st_sc1(c.rs, (nt <= nt1max ? OFF_HF + layer * 6 * MB + (tb * KSF + 2 * nt + kh) * 1024 : 0x20000000u) + lane * 16, hpack); };
      auto xw = [&](int t) { return L + 98304 + (t & 1) * 32768 + w * 2560; };
      auto xr = [&](int t) { return (const LAS char*)(L + 98304 + (t & 1) * 32768 + (w ^ 4) * 2560); };
      auto gelu = [&](auto ti, auto hi, const f32x4& xa, const f32x4& ta, const f32x4& tb2) {
        constexpr int t = decltype(ti)::value, hf = decltype(hi)::value;
        if constexpr (t == 0 && hf == 0) { f32x2 o = *(const LAS f32x2*)(L + 98304 + tl * 2560 + 2048 + lane * 8);
#pragma unroll
          for (int i = 1; i < 4; ++i) o += *(const LAS f32x2*)(L + 98304 + (tl + 2 * i) * 2560 + 2048 + lane * 8);
          ln_fin(o[0], o[1], mean, rstd); }
        const float C1 = -2.f * 0.7978845608028654f * 1.4426950408889634f, C2 = C1 * 0.044715f, nm = -mean * rstd;
        float x[4], v[4];
#define PIN4(a) asm volatile("" : "+v"(a[0]), "+v"(a[1]), "+v"(a[2]), "+v"(a[3]))
#pragma unroll
        for (int i = 0; i < 4; ++i) { const f32x4 tq = i < 2 ? ta : tb2; x[i] = __builtin_fmaf(tq[2 * (i & 1)], nm, tq[2 * (i & 1) + 1]); v[i] = keep[4 * hf + i] + xa[i]; }
        PIN4(x);
#pragma unroll
        for (int i = 0; i < 4; ++i) x[i] = __builtin_fmaf(v[i], rstd, x[i]);
        PIN4(x);
#pragma unroll
        for (int i = 0; i < 4; ++i) v[i] = x[i] * x[i];
        PIN4(v);
#pragma unroll
        for (int i = 0; i < 4; ++i) v[i] = __builtin_fmaf(v[i], C2, C1);
        PIN4(v);
#pragma unroll
        for (int i = 0; i < 4; ++i) v[i] = x[i] * v[i];
        PIN4(v);
#pragma unroll
        for (int i = 0; i < 4; ++i) v[i] = __builtin_amdgcn_exp2f(v[i]);
        PIN4(v);
#pragma unroll
        for (int i = 0; i < 4; ++i) v[i] = 1.f + v[i];
        PIN4(v);
#pragma unroll
        for (int i = 0; i < 4; ++i) v[i] = __builtin_amdgcn_rcpf(v[i]);
        PIN4(v);
#pragma unroll
        for (int i = 0; i < 4; ++i) v[i] = x[i] * v[i];
        PIN4(v);
#undef PIN4
        hpack[2 * hf] = cvtpk(v[0], v[1]); hpack[2 * hf + 1] = cvtpk(v[2], v[3]);
      };
      static_for<0, 6>([&](auto si) { constexpr int s = decltype(si)::value, t = s >> 1, kc = s & 1;
        bar_lgkm();
        f32x4 xa, ta, tb2;
        if constexpr (t >= 1) { xa = *(const LAS f32x4*)(xr(t - 1) + 1024 * kc + lane * 16); ta = *(const LAS f32x4*)(lT + (t - 1) * 256 + kc * 64); tb2 = *(const LAS f32x4*)(lT + (t - 1) * 256 + kc * 64 + 16); }
        bf16x8 af[8];
        if constexpr (t == 0) {
#pragma unroll
          for (int i = 0; i < 8; ++i) bq[kc * 8 + i] = *(const LAS bf16x8*)(lB + kc * 32768 + i * 1024); }
#pragma unroll
        for (int i = 0; i < 8; ++i) af[i] = *(const LAS bf16x8*)((s % 3 == 2 ? lA2 : lA + (s % 3) * 32768) + i * 1024);
        __builtin_amdgcn_sched_barrier(0);
        auto mm = [&]() {
          if constexpr (kc == 0) {
#pragma unroll
            for (int i = 0; i < 16; ++i) acc[i] = 0.f; }
#pragma unroll
          for (int i = 0; i < 8; ++i) acc = MFMA(af[i], bq[kc * 8 + i], acc); };
        if constexpr (t >= 1) {
          if (t == 2 && !has3) gelu(IC<t - 1>{}, IC<kc>{}, xa, ta, tb2);
          else {
            mm(); gelu(IC<t - 1>{}, IC<kc>{}, xa, ta, tb2);
#pragma unroll
            for (int k = 0; k < 8; ++k) { __builtin_amdgcn_sched_group_barrier(0x008, 1, 0); __builtin_amdgcn_sched_group_barrier(0x002, 6, 0); } }
        } else mm();
        __builtin_amdgcn_sched_barrier(0);
        if constexpr (kc == 1) {
          xsend(xw(t) + lane * 16, acc);
#pragma unroll
          for (int i = 0; i < 8; ++i) keep[i] = acc[i];
          if constexpr (t == 0) *(LAS f32x2*)(xw(0) + 2048 + lane * 8) = stat_quarter(sp);
          if constexpr (t >= 1) hstore(t - 1);
        }
      });
      bar_lgkm();
      if (has3) { const f32x4 x0 = *(const LAS f32x4*)(xr(2) + lane * 16), x1 = *(const LAS f32x4*)(xr(2) + 1024 + lane * 16);
        const f32x4 t0 = *(const LAS f32x4*)(lT + 512), t1 = *(const LAS f32x4*)(lT + 512 + 16), t2 = *(const LAS f32x4*)(lT + 512 + 64), t3 = *(const LAS f32x4*)(lT + 512 + 80);
        gelu(IC<2>{}, IC<0>{}, x0, t0, t1); gelu(IC<2>{}, IC<1>{}, x1, t2, t3); hstore(2); }
      group_sync<false>(c, layer * 4 + 2);
    }
    {
      const Ctx c = fresh(c0); const int lane = c.lane, w = c.w, g = c.g, j = c.j, tl = w & 1, ng = (w >> 1) & 1, kh = w >> 2, r = lane & 31, h = lane >> 5, tb = 2 * g + tl, fo = 16 * kh + 4 * h; char* ws = c.ws; (void)r; (void)fo; (void)tb; (void)ws; (void)ng;
      const LAS char* lA = (const LAS char*)lds + (lane ^ (kh << 4)) * 16 + (kh * 2 + ng) * 4096;
      const LAS char* lB = (const LAS char*)lds + 65536 + lane * 16 + (kh * 2 + tl) * 4096;
      const LAS char* lB4 = lB + 65536; asm volatile("" : "+v"(lB4));
      f32x16 acc; u32x2 bv0, bv1;
#pragma unroll
      for (int i = 0; i < 16; ++i) acc[i] = 0.f;
      bf16x8 bf[2][4], af[2][4];
      static_for<0, 11>([&](auto ci) { constexpr int ch = decltype(ci)::value; constexpr int n = ch < 10 ? 4 : 3;
        bar_lgkm();
#pragma unroll
        for (int i = 0; i < n; ++i) { bf[ch & 1][i] = *(const LAS bf16x8*)((ch % 6 >= 4 ? lB4 + (ch % 6 - 4) * 16384 : lB + (ch % 6) * 16384) + i * 1024); af[ch & 1][i] = *(const LAS bf16x8*)(lA + (ch % 4) * 16384 + i * 1024); }
        if constexpr (ch == 10) { const int f0 = (2 * j + ng) * 32 + fo; bv0 = gld<u32x2>(b2 + f0); bv1 = gld<u32x2>(b2 + f0 + 8); }
        __builtin_amdgcn_sched_barrier(0);
        if constexpr (ch >= 1) {
#pragma unroll
          for (int i = 0; i < 4; ++i) acc = MFMA(af[(ch - 1) & 1][i], bf[(ch - 1) & 1][i], acc); }
        __builtin_amdgcn_sched_barrier(0);
      });
#pragma unroll
      for (int i = 0; i < 3; ++i) acc = MFMA(af[0][i], bf[0][i], acc);
      LAS char* xw = L + w * 2048; const LAS char* xr = L + (w ^ 4) * 2048;
      xsend(xw + lane * 16, acc);
      bar_lgkm();
      float v[8]; xfin(xr + lane * 16, acc, v);
#pragma unroll
      for (int i = 0; i < 4; ++i) { const unsigned bu = (i < 2 ? bv0 : bv1)[i & 1]; v[2 * i] += bflo(resid[i]) + bflo(bu); v[2 * i + 1] += bfhi(resid[i]) + bfhi(bu); }
      resid = pack8(v);
      if (layer == 0) {
        st_sc1(c.rs, OFF_X2F + (tb * KSD + 2 * (2 * j + ng) + kh) * 1024 + lane * 16, resid);
        stat_store(c, 2, 2 * (2 * j + ng) + kh, tb, resid);
        group_sync(c, 3);
      } else {
        auto s0 = __builtin_amdgcn_permlane32_swap(resid[0], resid[2], false, false); auto s1 = __builtin_amdgcn_permlane32_swap(resid[1], resid[3], false, false);
        *(GAS u32x4*)(out + (size_t)(tb * 32 + r) * 512 + (2 * j + ng) * 32 + 16 * kh + 8 * h) = u32x4{s0[0], s1[0], s0[1], s1[1]};
      }
    }
    if (layer == 0) {
      const Ctx c = fresh(c0); const int lane = c.lane, w = c.w, g = c.g, j = c.j, tl = w & 1, ng = (w >> 1) & 1, kh = w >> 2, r = lane & 31, h = lane >> 5, tb = 2 * g + tl, fo = 16 * kh + 4 * h; char* ws = c.ws;
      f32x16 acc; f32x4 cs0, cs1; u32x2 sp[4]; float mean, rstd;
      stat_load(c, 2, tb, sp);
      const float* cs = (const float*)(ws + OFF_CSQ);
      GAS unsigned* kvfl = (GAS unsigned*)(ws + OFF_KVFLAG);
      auto ldcs = [&]() { const int f0 = (2 * j + ng) * 32 + fo; cs0 = gld<f32x4>(cs + f0); cs1 = gld<f32x4>(cs + f0 + 8); };
      auto stats = [&](int off, int stride) { f32x2 o = *(const LAS f32x2*)(L + off + tl * stride + lane * 8);
#pragma unroll
        for (int i = 1; i < 4; ++i) o += *(const LAS f32x2*)(L + off + (tl + 2 * i) * stride + lane * 8);
        ln_fin(o[0], o[1], mean, rstd); };
      if (j < 2) {
        const bool kvme = tl == j; f32x4 ck0, ck1; float csd;
        ck0 = gld<f32x4>(cs + 528 + 4 * h); ck1 = gld<f32x4>(cs + 528 + 8 + 4 * h); csd = gld<float>(cs + 512 + (r & 15)); ldcs();
        if (!kvme) mm_kv<0>(c, lds, acc); else if (ng == 0) mm_kv<1>(c, lds, acc); else mm_kv<2>(c, lds, acc);
        if (kvme) xsend(L + 65536 + kh * 16384 + ng * 2048 + lane * 16, acc);
        *(LAS f32x2*)(L + 65536 + 4096 + w * 512 + lane * 8) = stat_quarter(sp);
        bar_lgkm();
        stats(65536 + 4096, 512);
        if (kvme) {
          float v[8]; xfin(L + 65536 + (kh ^ 1) * 16384 + ng * 2048 + lane * 16, acc, v);
          if (ng == 0) { if (kh == 1) {
              const float b3 = -mean * rstd;
#pragma unroll
              for (int i = 0; i < 4; ++i) { v[i] = v[i] * rstd + b3 * ck0[i]; v[4 + i] = v[4 + i] * rstd + b3 * ck1[i]; }
              st_sc1(c.rs, OFF_KF + 65536 + tb * 1024 + lane * 16, pack8(v)); }
          } else {
#pragma unroll
            for (int i = 0; i < 8; ++i) { const int t = 16 * kh + 8 * (i >> 2) + 4 * h + (i & 3); const float m = __int_as_float(__builtin_amdgcn_ds_bpermute(t * 4, __float_as_int(mean))), rs = __int_as_float(__builtin_amdgcn_ds_bpermute(t * 4, __float_as_int(rstd))); v[i] = (v[i] - m * csd) * rs; }
            if (r < 16) st_sc1(c.rs, OFF_VF + 65536 + tb * 1024 + kh * 512 + (h * 16 + r) * 16, pack8(v));
          }
        }
        drain_bar();
        if (w == 1 && lane < 8) __hip_atomic_store(kvfl + lane * 64 + 2 * g + j, 1u, RLX_AGENT);
        mm_q16(c, lds, acc);
      } else {
        mm22(c, lds, 98304, acc, ldcs);
        *(LAS f32x2*)(L + 65536 + 16384 + w * 512 + lane * 8) = stat_quarter(sp);
      }
      xsend(L + 65536 + w * 2048 + lane * 16, acc);
      bar_lgkm();
      if (j >= 2) stats(65536 + 16384, 512);
      float v[8]; xfin(L + 65536 + (w ^ 4) * 2048 + lane * 16, acc, v);
      { const float a = rstd * (0.25f * 1.4426950408889634f), bb = -mean * a;
#pragma unroll
        for (int i = 0; i < 4; ++i) { v[i] = v[i] * a + bb * cs0[i]; v[4 + i] = v[4 + i] * a + bb * cs1[i]; } }
      *(LAS u32x4*)(L + AL_Q + (tl * 4 + 2 * ng + kh) * 1024 + lane * 16) = pack8(v);
      if (w == 0) { GAS unsigned* mine = kvfl + (blockIdx.x & 7) * 64 + lane;
        for (unsigned s = 0; s < SPIN_CAP; ++s) { if (__all(__hip_atomic_load(mine, RLX_AGENT) != 0u)) break; } }
      bar_lgkm();
    }
  }
}

extern "C" void kernel_launch(void* const* d_in, const int* in_sizes, int n_in, void* d_out, int out_size, void* d_ws, size_t ws_size, hipStream_t stream) {
  const unsigned short* x = (const unsigned short*)d_in[0];
  const unsigned short* wq = (const unsigned short*)d_in[1];
  const unsigned short* wk = (const unsigned short*)d_in[2];
  const unsigned short* wv = (const unsigned short*)d_in[3];
  const unsigned short* wo = (const unsigned short*)d_in[4];
  const unsigned short* w1 = (const unsigned short*)d_in[6];
  const unsigned short* b1 = (const unsigned short*)d_in[7];
  const unsigned short* w2 = (const unsigned short*)d_in[8];
  const unsigned short* b2 = (const unsigned short*)d_in[9];
  char* ws = (char*)d_ws + ((ws_size - WS_USED) & ~(size_t)0xfffff);
  (void)hipFuncSetAttribute((const void*)qkv0_kernel, hipFuncAttributeMaxDynamicSharedMemorySize, LDS_BYTES);
  (void)hipFuncSetAttribute((const void*)mega_kernel, hipFuncAttributeMaxDynamicSharedMemorySize, LDS_BYTES);
  qkv0_kernel<<<256, 768, LDS_BYTES, stream>>>(x, wq, wk, wv, w1, b1, ws);
  mega_kernel<<<256, 768, LDS_BYTES, stream>>>(ws, x, wq, wk, wv, wo, w1, w2, b2, (unsigned short*)d_out);
}
```
